```python
import torch

def _softmax(x, axis=-1):
    return torch.softmax(x, dim=axis)
def _gelu(x):
    return torch.nn.functional.gelu(x, approximate="tanh")
def _silu(x):
    return torch.nn.functional.silu(x)
def _sigmoid(x):
    return torch.sigmoid(x)
def _layernorm(x, axis=-1, eps=1e-5):
    m = x.mean(dim=axis, keepdim=True)
    v = x.var(dim=axis, keepdim=True, unbiased=False)
    return (x - m) / torch.sqrt(v + eps)
def _scatter_add(tgt, idx, src, axis=0):
    return tgt.clone().index_add_(axis, idx, src)
def _segment_sum(src, seg, n):
    out = torch.zeros((n,) + src.shape[1:], dtype=src.dtype)
    return out.index_add_(0, seg, src)
def _pad(x, pw):
    flat = tuple(v for pair in reversed(pw) for v in pair)
    return torch.nn.functional.pad(x, flat)

kernel_name = 'recurrent_scan__sliding_local_dilated__dense_activation_squared_relu__prompt'

def setup_inputs():

    _w = (lambda a: (lambda t: t.to(torch.bfloat16) if t.is_floating_point() else t)(torch.as_tensor(a)))
    def _A_setup():
        B, S, D = 1, 2048, 512
        return {
            "x": _w((lambda *s: torch.randn(*s, dtype=torch.bfloat16) * 0.05)(B, S, D)),
            "wqkv": _w((lambda *s: torch.randn(*s, dtype=torch.bfloat16) * 0.05)(D, 3 * D)),
            "wo": _w((lambda *s: torch.randn(*s, dtype=torch.bfloat16) * 0.05)(D, D)),
        }
    _A_in = _A_setup()

    def _B_setup():
        B, S, D, F = 1, 2048, 512, 1792
        return {
            "x": _w((lambda *s: torch.randn(*s, dtype=torch.bfloat16) * 0.05)(B, S, D)),
            "w1": _w((lambda *s: torch.randn(*s, dtype=torch.bfloat16) * 0.05)(D, F)),
            "b1": _w((lambda *s: torch.randn(*s, dtype=torch.bfloat16) * 0.4)(F)),
            "w2": _w((lambda *s: torch.randn(*s, dtype=torch.bfloat16) * 0.05)(F, D)),
            "b2": _w((lambda *s: torch.randn(*s, dtype=torch.bfloat16) * 0.8)(D)),
        }
    _B_in = _B_setup()

    return {
        "A_x": _A_in["x"],
        "A_wqkv": _A_in["wqkv"],
        "A_wo": _A_in["wo"],
        "B_x": _B_in["x"],
        "B_w1": _B_in["w1"],
        "B_b1": _B_in["b1"],
        "B_w2": _B_in["w2"],
        "B_b2": _B_in["b2"],
    }

def reference(A_x, A_wqkv, A_wo, B_x, B_w1, B_b1, B_w2, B_b2):
    def _A_ref(x, wqkv, wo):
        B, S, D = x.shape
        H = 32; Dh = D // H
        W, dil = 8, 2
        qkv = (lambda x, *d: x.permute(*d))((x @ wqkv).reshape(B, S, 3, H, Dh), 2, 0, 3, 1, 4)
        q, k, v = qkv[0], qkv[1], qkv[2]
        scale = Dh ** -0.5
        pos = torch.arange(S)
        off = torch.arange(W) * dil
        idx = pos[:, None] - off[None, :]
        mask = idx >= 0
        idx_c = torch.where(mask, idx, 0).reshape(-1)
        kg = k[:, :, idx_c, :].reshape(B, H, S, W, Dh)
        vg = v[:, :, idx_c, :].reshape(B, H, S, W, Dh)
        logits = torch.einsum("bhsd,bhswd->bhsw", q, kg) * scale
        logits = torch.where(mask[None, None, :, :], logits, -1e9)
        attn = _softmax(logits, axis=-1)
        o = torch.einsum("bhsw,bhswd->bhsd", attn, vg)
        return (lambda x, *d: x.permute(*d))(o, 0, 2, 1, 3).reshape(B, S, D) @ wo

    def _B_ref(x, w1, b1, w2, b2):
        h = torch.einsum("bsd,df->bsf", x, w1) + b1
        zero = torch.zeros_like(h)
        r = torch.where(h > zero, h, zero)
        h = r * r
        out = torch.einsum("bsf,fd->bsd", h, w2) + b2
        return out

    x = A_x
    xn = _layernorm(x, axis=-1)
    s = _A_ref(xn, A_wqkv, A_wo)
    g = _silu(_B_ref(xn, B_w1, B_b1, B_w2, B_b2))
    return x + s * g
```

```cpp
#include <hip/hip_runtime.h>
#include <hip/hip_bf16.h>
typedef unsigned short u16;
__device__ __forceinline__ float bf2f(u16 v){ return __uint_as_float(((unsigned)v)<<16); }
__device__ __forceinline__ u16 f2bf(float f){ __hip_bfloat16 b = __float2bfloat16(f); return *(u16*)&b; }
__device__ __forceinline__ float rbf(float f){ return bf2f(f2bf(f)); }
constexpr int S=2048, D=512, F=1792, H=32, DH=16;

__global__ void k_ln(const u16* x, float* xn){
  int row = blockIdx.x; int t = threadIdx.x;
  float v[8]; float s=0;
  for(int i=0;i<8;i++){ v[i]=bf2f(x[row*D+t*8+i]); s+=v[i]; }
  for(int o=32;o>0;o>>=1) s+=__shfl_xor(s,o);
  float m=s/D; float q=0;
  for(int i=0;i<8;i++){ float d=v[i]-m; q+=d*d; }
  for(int o=32;o>0;o>>=1) q+=__shfl_xor(q,o);
  float r = rsqrtf(q/D+1e-5f);
  for(int i=0;i<8;i++) xn[row*D+t*8+i]=(v[i]-m)*r;
}
__global__ void k_gemm(const float* A, const u16* B, float* C, int N, int K){
  __shared__ float As[16][17]; __shared__ float Bs[16][17];
  int tx=threadIdx.x&15, ty=threadIdx.x>>4;
  int r=blockIdx.y*16+ty, c=blockIdx.x*16+tx; float acc=0;
  for(int k0=0;k0<K;k0+=16){
    As[ty][tx]=A[r*K+k0+tx]; Bs[ty][tx]=bf2f(B[(k0+ty)*N+c]);
    __syncthreads();
    for(int k=0;k<16;k++) acc+=As[ty][k]*Bs[k][tx];
    __syncthreads();
  }
  C[r*N+c]=acc;
}
__global__ void k_attn(const float* qkv, float* o){
  int idx = blockIdx.x*blockDim.x+threadIdx.x; int s=idx/H, h=idx%H;
  float lg[8]; float mx=-1e30f;
  for(int w=0;w<8;w++){ int p=s-2*w; float a=-1e9f; if(p>=0){ a=0; for(int d=0;d<16;d++) a+=rbf(qkv[s*1536+h*16+d])*rbf(qkv[p*1536+512+h*16+d]); a*=0.25f;} lg[w]=a; mx=fmaxf(mx,a);}
  float sum=0; for(int w=0;w<8;w++){ lg[w]=__expf(lg[w]-mx); sum+=lg[w]; }
  for(int d=0;d<16;d++){ float a=0; for(int w=0;w<8;w++){ int p=s-2*w; if(p<0)p=0; a+=rbf(lg[w]/sum)*rbf(qkv[p*1536+1024+h*16+d]); } o[s*D+h*16+d]=rbf(a);}
}
__global__ void k_act(float* h, const u16* b1){ int i=blockIdx.x*blockDim.x+threadIdx.x; float v=h[i]+bf2f(b1[i%F]); v=v>0?v:0; h[i]=rbf(v*v);}
__global__ void k_fin(const u16* x, const float* s, const float* o2, const u16* b2, u16* out){ int i=blockIdx.x*blockDim.x+threadIdx.x; float g=o2[i]+bf2f(b2[i%D]); g=g/(1.f+__expf(-g)); out[i]=f2bf(bf2f(x[i])+s[i]*g);}

extern "C" void kernel_launch(void* const* d_in, const int* in_sizes, int n_in, void* d_out, int out_size, void* d_ws, size_t ws_size, hipStream_t stream) {
  const u16* x=(const u16*)d_in[0]; const u16* wqkv=(const u16*)d_in[1]; const u16* wo=(const u16*)d_in[2];
  const u16* w1=(const u16*)d_in[4]; const u16* b1=(const u16*)d_in[5]; const u16* w2=(const u16*)d_in[6]; const u16* b2=(const u16*)d_in[7];
  float* ws=(float*)d_ws; float* xn=ws; float* qkv=xn+S*D; float* o=qkv+S*1536; float* s=o+S*D; float* h=s+S*D; float* o2=h+S*F;
  k_ln<<<S,64,0,stream>>>(x,xn);
  k_gemm<<<dim3(1536/16,S/16),256,0,stream>>>(xn,wqkv,qkv,1536,D);
  k_attn<<<S*H/256,256,0,stream>>>(qkv,o);
  k_gemm<<<dim3(D/16,S/16),256,0,stream>>>(o,wo,s,D,D);
  k_gemm<<<dim3(F/16,S/16),256,0,stream>>>(xn,w1,h,F,D);
  k_act<<<S*F/256,256,0,stream>>>(h,b1);
  k_gemm<<<dim3(D/16,S/16),256,0,stream>>>(h,w2,o2,D,F);
  k_fin<<<S*D/256,256,0,stream>>>(x,s,o2,b2,(u16*)d_out);
}
```

```cpp
#include <hip/hip_runtime.h>
#include <hip/hip_bf16.h>
typedef unsigned short u16;
__device__ __forceinline__ float bf2f(u16 v){ return __uint_as_float(((unsigned)v)<<16); }
__device__ __forceinline__ u16 f2bf(float f){ __hip_bfloat16 b = __float2bfloat16(f); return *(u16*)&b; }
__device__ __forceinline__ float rbf(float f){ return bf2f(f2bf(f)); }
constexpr int S=2048, D=512, F=1792, H=32, DH=16;

__global__ void k_ln(const u16* x, float* xn){
  int row = blockIdx.x; int t = threadIdx.x;
  float v[8]; float s=0;
  for(int i=0;i<8;i++){ v[i]=bf2f(x[row*D+t*8+i]); s+=v[i]; }
  for(int o=32;o>0;o>>=1) s+=__shfl_xor(s,o);
  float m=s/D; float q=0;
  for(int i=0;i<8;i++){ float d=v[i]-m; q+=d*d; }
  for(int o=32;o>0;o>>=1) q+=__shfl_xor(q,o);
  float sd = rbf(sqrtf(rbf(rbf(q/D)+1e-5f))); m = rbf(m);
  for(int i=0;i<8;i++) xn[row*D+t*8+i]=rbf(rbf(v[i]-m)/sd);
}
__global__ void k_gemm(const float* A, const u16* B, float* C, int N, int K){
  __shared__ float As[16][17]; __shared__ float Bs[16][17];
  int tx=threadIdx.x&15, ty=threadIdx.x>>4;
  int r=blockIdx.y*16+ty, c=blockIdx.x*16+tx; float acc=0;
  for(int k0=0;k0<K;k0+=16){
    As[ty][tx]=A[r*K+k0+tx]; Bs[ty][tx]=bf2f(B[(k0+ty)*N+c]);
    __syncthreads();
    for(int k=0;k<16;k++) acc+=As[ty][k]*Bs[k][tx];
    __syncthreads();
  }
  C[r*N+c]=acc;
}
__global__ void k_attn(const float* qkv, float* o){
  int idx = blockIdx.x*blockDim.x+threadIdx.x; int s=idx/H, h=idx%H;
  float lg[8]; float mx=-1e30f;
  for(int w=0;w<8;w++){ int p=s-2*w; float a=-1e9f; if(p>=0){ a=0; for(int d=0;d<16;d++) a+=rbf(qkv[s*1536+h*16+d])*rbf(qkv[p*1536+512+h*16+d]); a=rbf(rbf(a)*0.25f);} lg[w]=a; mx=fmaxf(mx,a);}
  float sum=0; for(int w=0;w<8;w++){ lg[w]=__expf(lg[w]-mx); sum+=lg[w]; }
  for(int d=0;d<16;d++){ float a=0; for(int w=0;w<8;w++){ int p=s-2*w; if(p<0)p=0; a+=rbf(lg[w]/sum)*rbf(qkv[p*1536+1024+h*16+d]); } o[s*D+h*16+d]=rbf(a);}
}
__global__ void k_act(float* h, const u16* b1){ int i=blockIdx.x*blockDim.x+threadIdx.x; float v=rbf(rbf(h[i])+bf2f(b1[i%F])); v=v>0?v:0; h[i]=rbf(v*v);}
__global__ void k_fin(const u16* x, const float* s, const float* o2, const u16* b2, u16* out){ int i=blockIdx.x*blockDim.x+threadIdx.x; float g=rbf(rbf(o2[i])+bf2f(b2[i%D])); g=rbf(g/(1.f+__expf(-g))); out[i]=f2bf(bf2f(x[i])+rbf(rbf(s[i])*g));}

extern "C" void kernel_launch(void* const* d_in, const int* in_sizes, int n_in, void* d_out, int out_size, void* d_ws, size_t ws_size, hipStream_t stream) {
  const u16* x=(const u16*)d_in[0]; const u16* wqkv=(const u16*)d_in[1]; const u16* wo=(const u16*)d_in[2];
  const u16* w1=(const u16*)d_in[4]; const u16* b1=(const u16*)d_in[5]; const u16* w2=(const u16*)d_in[6]; const u16* b2=(const u16*)d_in[7];
  float* ws=(float*)d_ws; float* xn=ws; float* qkv=xn+S*D; float* o=qkv+S*1536; float* s=o+S*D; float* h=s+S*D; float* o2=h+S*F;
  k_ln<<<S,64,0,stream>>>(x,xn);
  k_gemm<<<dim3(1536/16,S/16),256,0,stream>>>(xn,wqkv,qkv,1536,D);
  k_attn<<<S*H/256,256,0,stream>>>(qkv,o);
  k_gemm<<<dim3(D/16,S/16),256,0,stream>>>(o,wo,s,D,D);
  k_gemm<<<dim3(F/16,S/16),256,0,stream>>>(xn,w1,h,F,D);
  k_act<<<S*F/256,256,0,stream>>>(h,b1);
  k_gemm<<<dim3(D/16,S/16),256,0,stream>>>(h,w2,o2,D,F);
  k_fin<<<S*D/256,256,0,stream>>>(x,s,o2,b2,(u16*)d_out);
}
```

```cpp
#include <hip/hip_runtime.h>
#include <hip/hip_bf16.h>
typedef unsigned short u16;
__device__ __forceinline__ float bf2f(u16 v){ return __uint_as_float(((unsigned)v)<<16); }
__device__ __forceinline__ u16 f2bf(float f){ __hip_bfloat16 b = __float2bfloat16(f); return *(u16*)&b; }
__device__ __forceinline__ float rbf(float f){ return bf2f(f2bf(f)); }
constexpr int S=2048, D=512, F=1792, H=32, DH=16;

__global__ void k_ln(const u16* x, float* xn){
  int row = blockIdx.x; int t = threadIdx.x;
  float v[8]; float s=0;
  for(int i=0;i<8;i++){ v[i]=bf2f(x[row*D+t*8+i]); s+=v[i]; }
  for(int o=32;o>0;o>>=1) s+=__shfl_xor(s,o);
  float m=s/D; float q=0;
  for(int i=0;i<8;i++){ float d=v[i]-m; q+=d*d; }
  for(int o=32;o>0;o>>=1) q+=__shfl_xor(q,o);
  float r = rsqrtf(q/D+1e-5f);
  for(int i=0;i<8;i++) xn[row*D+t*8+i]=(v[i]-m)*r;
}
__global__ void k_gemm(const float* A, const u16* B, float* C, int N, int K){
  __shared__ float As[16][17]; __shared__ float Bs[16][17];
  int tx=threadIdx.x&15, ty=threadIdx.x>>4;
  int r=blockIdx.y*16+ty, c=blockIdx.x*16+tx; float acc=0;
  for(int k0=0;k0<K;k0+=16){
    As[ty][tx]=A[r*K+k0+tx]; Bs[ty][tx]=bf2f(B[(k0+ty)*N+c]);
    __syncthreads();
    for(int k=0;k<16;k++) acc+=As[ty][k]*Bs[k][tx];
    __syncthreads();
  }
  C[r*N+c]=acc;
}
__global__ void k_attn(const float* qkv, float* o){
  int idx = blockIdx.x*blockDim.x+threadIdx.x; int s=idx/H, h=idx%H;
  float lg[8]; float mx=-1e30f;
  for(int w=0;w<8;w++){ int p=s-2*w; float a=-1e9f; if(p>=0){ a=0; for(int d=0;d<16;d++) a+=rbf(qkv[s*1536+h*16+d])*rbf(qkv[p*1536+512+h*16+d]); a*=0.25f;} lg[w]=a; mx=fmaxf(mx,a);}
  float sum=0; for(int w=0;w<8;w++){ lg[w]=__expf(lg[w]-mx); sum+=lg[w]; }
  for(int d=0;d<16;d++){ float a=0; for(int w=0;w<8;w++){ int p=s-2*w; if(p<0)p=0; a+=rbf(lg[w]/sum)*rbf(qkv[p*1536+1024+h*16+d]); } o[s*D+h*16+d]=rbf(a);}
}
__global__ void k_act(float* h, const u16* b1){ int i=blockIdx.x*blockDim.x+threadIdx.x; float v=h[i]+bf2f(b1[i%F]); v=v>0?v:0; h[i]=rbf(v*v);}
__global__ void k_fin(const u16* x, const float* s, const float* o2, const u16* b2, u16* out){ int i=blockIdx.x*blockDim.x+threadIdx.x; float g=o2[i]+bf2f(b2[i%D]); g=g/(1.f+__expf(-g)); out[i]=f2bf(bf2f(x[i])+s[i]*g);}


__global__ void k_probe(const char* p, unsigned stride, int n, unsigned* sink){
  unsigned acc=0;
  for(int i=0;i<n;i++){
    unsigned v=*(const volatile unsigned*)(p+(size_t)i*stride+(acc&0));
    asm volatile("s_waitcnt vmcnt(0)" : "+v"(v) :: "memory");
    acc+=v;
    asm volatile("" : "+v"(acc));
  }
  if(acc==0x13572468 && n<0) sink[0]=acc;
}
extern "C" void kernel_launch(void* const* d_in, const int* in_sizes, int n_in, void* d_out, int out_size, void* d_ws, size_t ws_size, hipStream_t stream) {
  const u16* x=(const u16*)d_in[0]; const u16* wqkv=(const u16*)d_in[1]; const u16* wo=(const u16*)d_in[2];
  const u16* w1=(const u16*)d_in[4]; const u16* b1=(const u16*)d_in[5]; const u16* w2=(const u16*)d_in[6]; const u16* b2=(const u16*)d_in[7];
  float* ws=(float*)d_ws; float* xn=ws; float* qkv=xn+S*D; float* o=qkv+S*1536; float* s=o+S*D; float* h=s+S*D; float* o2=h+S*F;
  k_probe<<<1,64,0,stream>>>((const char*)(d_in[0])+0,512,1,(unsigned*)d_ws);
  k_ln<<<S,64,0,stream>>>(x,xn);
  k_gemm<<<dim3(1536/16,S/16),256,0,stream>>>(xn,wqkv,qkv,1536,D);
  k_attn<<<S*H/256,256,0,stream>>>(qkv,o);
  k_gemm<<<dim3(D/16,S/16),256,0,stream>>>(o,wo,s,D,D);
  k_gemm<<<dim3(F/16,S/16),256,0,stream>>>(xn,w1,h,F,D);
  k_act<<<S*F/256,256,0,stream>>>(h,b1);
  k_gemm<<<dim3(D/16,S/16),256,0,stream>>>(h,w2,o2,D,F);
  k_fin<<<S*D/256,256,0,stream>>>(x,s,o2,b2,(u16*)d_out);
}
```

```cpp
#include <hip/hip_runtime.h>
#include <hip/hip_bf16.h>
typedef unsigned short u16;
__device__ __forceinline__ float bf2f(u16 v){ return __uint_as_float(((unsigned)v)<<16); }
__device__ __forceinline__ u16 f2bf(float f){ __hip_bfloat16 b = __float2bfloat16(f); return *(u16*)&b; }
__device__ __forceinline__ float rbf(float f){ return bf2f(f2bf(f)); }
constexpr int S=2048, D=512, F=1792, H=32, DH=16;

__global__ void k_ln(const u16* x, float* xn){
  int row = blockIdx.x; int t = threadIdx.x;
  float v[8]; float s=0;
  for(int i=0;i<8;i++){ v[i]=bf2f(x[row*D+t*8+i]); s+=v[i]; }
  for(int o=32;o>0;o>>=1) s+=__shfl_xor(s,o);
  float m=s/D; float q=0;
  for(int i=0;i<8;i++){ float d=v[i]-m; q+=d*d; }
  for(int o=32;o>0;o>>=1) q+=__shfl_xor(q,o);
  float r = rsqrtf(q/D+1e-5f);
  for(int i=0;i<8;i++) xn[row*D+t*8+i]=(v[i]-m)*r;
}
__global__ void k_gemm(const float* A, const u16* B, float* C, int N, int K){
  __shared__ float As[16][17]; __shared__ float Bs[16][17];
  int tx=threadIdx.x&15, ty=threadIdx.x>>4;
  int r=blockIdx.y*16+ty, c=blockIdx.x*16+tx; float acc=0;
  for(int k0=0;k0<K;k0+=16){
    As[ty][tx]=A[r*K+k0+tx]; Bs[ty][tx]=bf2f(B[(k0+ty)*N+c]);
    __syncthreads();
    for(int k=0;k<16;k++) acc+=As[ty][k]*Bs[k][tx];
    __syncthreads();
  }
  C[r*N+c]=acc;
}
__global__ void k_attn(const float* qkv, float* o){
  int idx = blockIdx.x*blockDim.x+threadIdx.x; int s=idx/H, h=idx%H;
  float lg[8]; float mx=-1e30f;
  for(int w=0;w<8;w++){ int p=s-2*w; float a=-1e9f; if(p>=0){ a=0; for(int d=0;d<16;d++) a+=rbf(qkv[s*1536+h*16+d])*rbf(qkv[p*1536+512+h*16+d]); a*=0.25f;} lg[w]=a; mx=fmaxf(mx,a);}
  float sum=0; for(int w=0;w<8;w++){ lg[w]=__expf(lg[w]-mx); sum+=lg[w]; }
  for(int d=0;d<16;d++){ float a=0; for(int w=0;w<8;w++){ int p=s-2*w; if(p<0)p=0; a+=rbf(lg[w]/sum)*rbf(qkv[p*1536+1024+h*16+d]); } o[s*D+h*16+d]=rbf(a);}
}
__global__ void k_act(float* h, const u16* b1){ int i=blockIdx.x*blockDim.x+threadIdx.x; float v=h[i]+bf2f(b1[i%F]); v=v>0?v:0; h[i]=rbf(v*v);}
__global__ void k_fin(const u16* x, const float* s, const float* o2, const u16* b2, u16* out){ int i=blockIdx.x*blockDim.x+threadIdx.x; float g=o2[i]+bf2f(b2[i%D]); g=g/(1.f+__expf(-g)); out[i]=f2bf(bf2f(x[i])+s[i]*g);}


typedef __attribute__((ext_vector_type(4))) unsigned u32x4;
__device__ __forceinline__ void spin_ticks(unsigned long long n){ unsigned long long r0=__builtin_amdgcn_s_memrealtime(); if(n>400000) n=400000; while(__builtin_amdgcn_s_memrealtime()-r0<n) __builtin_amdgcn_s_sleep(8); }
__global__ void k_clk(unsigned* sink){
  unsigned long long r0=__builtin_amdgcn_s_memrealtime(), c0=__builtin_amdgcn_s_memtime();
  while(__builtin_amdgcn_s_memrealtime()-r0<1000);
  unsigned long long c1=__builtin_amdgcn_s_memtime();
  spin_ticks((c1-c0)*4);
}
__global__ void k_bw(const u32x4* p, size_t n, unsigned* sink){ size_t i=blockIdx.x*(size_t)blockDim.x+threadIdx.x; u32x4 a={0,0,0,0}; for(;i<n;i+=(size_t)gridDim.x*blockDim.x) a^=p[i]; if(a.x==0x1234567&&a.y==0x7654321&&a.z==0x1111&&a.w==0x777) sink[0]=a.z; }
__global__ __launch_bounds__(256) void k_rec(unsigned long long* ts, const u32x4* x){
  unsigned long long t0,t1,t2,r0;
  asm volatile("s_memrealtime %1\n\ts_memtime %0\n\ts_waitcnt lgkmcnt(0)" : "=s"(t0), "=s"(r0) :: "memory");
  asm volatile("s_memtime %0\n\ts_waitcnt lgkmcnt(0)" : "=s"(t1) :: "memory");
  u32x4 a = x[blockIdx.x*512+threadIdx.x];
  asm volatile("s_waitcnt vmcnt(0)\n\ts_memtime %0\n\ts_waitcnt lgkmcnt(0)" : "=s"(t2), "+v"(a) :: "memory");
  if(threadIdx.x==0){ ts[blockIdx.x*4]=r0; ts[blockIdx.x*4+1]=t1-t0; ts[blockIdx.x*4+2]=t2-t1; }
}
__global__ void k_rep(const unsigned long long* ts, int mode){
  unsigned long long v=0;
  if(mode==0){ unsigned long long mn=~0ull,mx=0; for(int i=0;i<256;i++){ unsigned long long r=ts[i*4]; mn=r<mn?r:mn; mx=r>mx?r:mx;} v=(mx-mn)*1000; }
  else if(mode==3){ unsigned long long mn=~0ull; for(int i=0;i<256;i++){ unsigned long long r=ts[i*4]; mn=r<mn?r:mn;} v=(ts[0]-mn)*1000; }
  else { unsigned long long s=0; for(int i=0;i<256;i++) s+=ts[i*4+mode]; v=s*100/256; }
  spin_ticks(v);
}
extern "C" void kernel_launch(void* const* d_in, const int* in_sizes, int n_in, void* d_out, int out_size, void* d_ws, size_t ws_size, hipStream_t stream) {
  const u16* x=(const u16*)d_in[0]; const u16* wqkv=(const u16*)d_in[1]; const u16* wo=(const u16*)d_in[2];
  const u16* w1=(const u16*)d_in[4]; const u16* b1=(const u16*)d_in[5]; const u16* w2=(const u16*)d_in[6]; const u16* b2=(const u16*)d_in[7];
  float* ws=(float*)d_ws; float* xn=ws; float* qkv=xn+S*D; float* o=qkv+S*1536; float* s=o+S*D; float* h=s+S*D; float* o2=h+S*F;
  k_bw<<<2048,256,0,stream>>>((const u32x4*)d_ws,(size_t)(200u<<20)/16,(unsigned*)d_ws);
  k_ln<<<S,64,0,stream>>>(x,xn);
  k_gemm<<<dim3(1536/16,S/16),256,0,stream>>>(xn,wqkv,qkv,1536,D);
  k_attn<<<S*H/256,256,0,stream>>>(qkv,o);
  k_gemm<<<dim3(D/16,S/16),256,0,stream>>>(o,wo,s,D,D);
  k_gemm<<<dim3(F/16,S/16),256,0,stream>>>(xn,w1,h,F,D);
  k_act<<<S*F/256,256,0,stream>>>(h,b1);
  k_gemm<<<dim3(D/16,S/16),256,0,stream>>>(h,w2,o2,D,F);
  k_fin<<<S*D/256,256,0,stream>>>(x,s,o2,b2,(u16*)d_out);
}
```

```cpp
#include <hip/hip_runtime.h>

struct FusedArgs {
  const void *x, *ws, *wqkv, *w1, *wo, *w2, *b1, *b2, *out;
};

__global__ __attribute__((amdgpu_flat_work_group_size(256, 256), amdgpu_waves_per_eu(1, 1)))
void fused_block_kernel(FusedArgs args) {
  __shared__ __attribute__((aligned(16))) char smem[163840];
  asm volatile(
    "L_start:\n"
    "s_getpc_b64 s[20:21]\n"
    "s_load_dwordx2 s[4:5], s[0:1], 0x0\n"
    "s_lshr_b32 s48, s2, 7\n"
    "s_lshl_b32 s49, s48, 3\n"
    "s_add_u32 s49, s49, 16\n"
    "s_load_dwordx2 s[8:9], s[0:1], s49\n"
    "s_load_dwordx2 s[16:17], s[0:1], 0x8\n"
    "s_load_dwordx4 s[40:43], s[0:1], 0x20\n"
    "s_load_dwordx4 s[32:35], s[0:1], 0x30\n"
    "s_load_dwordx2 s[24:25], s[0:1], 0x40\n"
    "s_and_b32 s20, s20, 0xffffff00\n"
    "s_mov_b32 s22, L_end-L_start\n"
    "s_mov_b32 s23, 0x20000\n"
    "s_lshr_b32 s49, s2, 3\n"
    "s_lshl_b32 s50, s49, 4\n"
    "v_lshrrev_b32 v40, 4, v0\n"
    "v_add_lshl_u32 v40, v40, s50, 7\n"
    "buffer_load_dword v255, v40, s[20:23], 0 offen\n"
    "v_lshrrev_b32 v41, 6, v0\n"
    "v_and_b32 v1, 63, v0\n"
    "v_readfirstlane_b32 s3, v41\n"
    "s_and_b32 s30, s49, 7\n"
    "s_lshr_b32 s31, s2, 6\n"
    "s_bfe_u32 s50, s2, 0x10002\n"
    "s_lshl_b32 s52, s50, 3\n"
    "s_add_u32 s52, s52, s30\n"
    "s_and_b32 s51, s2, 3\n"
    "s_lshl_b32 s53, s51, 1\n"
    "s_and_b32 s56, s31, 1\n"
    "s_add_u32 s53, s53, s56\n"
    "s_lshl_b32 s56, s56, 1\n"
    "s_lshr_b32 s57, s51, 1\n"
    "s_add_u32 s56, s56, s57\n"
    "s_add_u32 s56, s56, s30\n"
    "s_and_b32 s56, s56, 3\n"
    "s_lshl_b32 s56, s56, 1\n"
    "s_xor_b32 s57, s50, s51\n"
    "s_and_b32 s57, s57, 1\n"
    "s_add_u32 s55, s56, s57\n"
    "s_lshl_b32 s54, s55, 1\n"
    "s_lshr_b32 s49, s52, 1\n"
    "s_lshl_b32 s62, s49, 18\n"
    "s_and_b32 s50, s52, 1\n"
    "s_lshl_b32 s50, s50, 10\n"
    "s_add_u32 s62, s62, s50\n"
    "s_lshl_b32 s59, s3, 12\n"
    "s_mul_i32 s60, s3, 3072\n"
    "v_lshrrev_b32 v42, 3, v1\n"
    "v_and_b32 v43, 7, v1\n"
    "v_lshrrev_b32 v44, 4, v1\n"
    "s_lshl_b32 s49, s48, 3\n"
    "s_add_u32 s63, s49, s53\n"
    "s_lshl_b32 s49, s63, 14\n"
    "s_add_u32 s49, s49, s59\n"
    "s_add_u32 s49, s49, s62\n"
    "v_lshrrev_b32 v45, 5, v1\n"
    "v_and_b32 v46, 31, v1\n"
    "v_lshlrev_b32 v45, 11, v45\n"
    "v_lshl_add_u32 v45, v46, 4, v45\n"
    "v_add_u32 v45, s49, v45\n"
    "v_xor_b32 v46, v43, v44\n"
    "v_lshlrev_b32 v46, 4, v46\n"
    "v_lshl_add_u32 v46, v42, 11, v46\n"
    "v_xor_b32 v47, 64, v46\n"
    "s_lshl_b32 s49, s3, 16\n"
    "s_add_u32 s49, s49, s62\n"
    "v_add_u32 v17, s49, v46\n"
    "s_add_u32 s49, s49, 15360\n"
    "v_add_u32 v18, s49, v47\n"
    "v_add_u32 v19, 30720, v17\n"
    "v_add_u32 v20, 30720, v18\n"
    "s_lshl_b32 s49, s48, 9\n"
    "s_add_u32 s49, s49, 3072\n"
    "s_lshl_b32 s29, s49, 5\n"
    "s_lshl_b32 s10, s49, 9\n"
    "s_add_u32 s10, s10, 2048\n"
    "s_mul_i32 s28, s48, 229248\n"
    "s_add_u32 s28, s28, 128\n"
    "s_mul_i32 s50, s48, 320\n"
    "s_add_u32 s50, s50, 128\n"
    "s_lshl_b32 s51, s48, 6\n"
    "s_mul_i32 s57, s48, -896\n"
    "s_mul_i32 s50, s50, s53\n"
    "s_add_u32 s50, s50, 2048\n"
    "s_bitcmp1_b32 s53, 0\n"
    "s_cselect_b32 s51, s51, 0\n"
    "s_cselect_b32 s58, 0, 384\n"
    "s_add_u32 s51, s51, s50\n"
    "s_add_u32 s58, s58, s50\n"
    "s_and_b32 s56, s3, 1\n"
    "s_lshl_b32 s56, s56, 2\n"
    "v_and_b32 v48, 1, v44\n"
    "v_lshlrev_b32 v48, 1, v48\n"
    "v_xor_b32 v48, s56, v48\n"
    "v_xor_b32 v48, v43, v48\n"
    "s_lshl_b32 s56, s3, 3\n"
    "v_add_u32 v49, s56, v42\n"
    "v_mul_u32_u24 v49, s49, v49\n"
    "v_lshl_add_u32 v49, v48, 4, v49\n"
    "v_add_u32 v21, s51, v49\n"
    "v_add_u32 v22, s57, v21\n"
    "v_add_u32 v23, s57, v22\n"
    "v_lshrrev_b32 v50, 2, v1\n"
    "s_lshl_b32 s56, s3, 4\n"
    "v_add_u32 v50, s56, v50\n"
    "v_mul_u32_u24 v50, 3584, v50\n"
    "v_and_b32 v51, 2, v44\n"
    "v_and_b32 v52, 3, v1\n"
    "v_xor_b32 v51, v51, v52\n"
    "v_lshl_add_u32 v50, v51, 4, v50\n"
    "v_add_u32 v50, s58, v50\n"
    "s_cmp_lt_u32 s52, 2\n"
    "s_cselect_b32 s50, 0, 16384\n"
    "s_sub_u32 s50, s62, s50\n"
    "s_cmp_eq_u32 s3, 1\n"
    "s_cselect_b32 s50, s50, 0x7fff0000\n"
    "s_cselect_b32 s51, 0, 1024\n"
    "s_add_u32 s51, s51, 17408\n"
    "v_add_u32 v51, s50, v47\n"
    "s_lshl_b32 s61, s3, 10\n"
    "s_add_u32 s61, s61, 16384\n"
    "s_cmp_lg_u32 s48, 0\n"
    "s_cselect_b32 s61, s61, s51\n"
    "s_cselect_b64 vcc, -1, 0\n"
    "v_cndmask_b32 v24, v51, v50, vcc\n"
    "s_mov_b32 s6, 2097152\n"
    "s_mov_b32 s7, 0x20000\n"
    "s_mov_b32 s11, 0x20000\n"
    "s_mov_b32 s18, 67108864\n"
    "s_mov_b32 s19, 0x20000\n"
    "s_mov_b32 s39, 0x20000\n"
    "s_cselect_b32 s38, s10, s6\n"
    "s_waitcnt lgkmcnt(0)\n"
    "buffer_load_dwordx4 v[56:59], v45, s[4:7], 0 offen\n"
    "buffer_load_dwordx4 v[60:63], v45, s[4:7], 0 offen offset:512\n"
    "s_sub_u32 s8, s8, 2048\n"
    "s_subb_u32 s9, s9, 0\n"
    "s_cmp_lg_u32 s48, 0\n"
    "s_cselect_b64 s[36:37], s[8:9], s[4:5]\n"
    "s_add_u32 s44, s16, 41943040\n"
    "s_addc_u32 s45, s17, 0\n"
    "s_lshl_b32 s57, s55, 7\n"
    "s_add_u32 m0, s59, 49152\n"
    "s_nop 0\n"
    "buffer_load_dwordx4 v17, s[4:7], s57 offen offset:0 lds\n"
    "buffer_load_dwordx4 v18, s[4:7], s57 offen offset:1024 lds\n"
    "buffer_load_dwordx4 v19, s[4:7], s57 offen offset:2048 lds\n"
    "s_add_u32 m0, s59, 49152\n"
    "s_mul_i32 s58, s55, s28\n"
    "buffer_load_dwordx4 v20, s[4:7], s57 offen offset:3072 lds\n"
    "s_add_u32 m0, s61, 49152\n"
    "s_nop 0\n"
    "buffer_load_dwordx4 v24, s[36:39], s58 offen offset:0 lds\n"
    "s_mul_i32 s56, s54, s29\n"
    "s_add_u32 m0, s60, 0\n"
    "s_nop 0\n"
    "buffer_load_dwordx4 v21, s[8:11], s56 offen offset:0 lds\n"
    "buffer_load_dwordx4 v22, s[8:11], s56 offen offset:1024 lds\n"
    "buffer_load_dwordx4 v23, s[8:11], s56 offen offset:2048 lds\n"
    "s_load_dwordx2 s[66:67], s[44:45], 0x0\n"
    "s_add_u32 s54, s54, 1\n"
    "s_and_b32 s54, s54, 15\n"
    "s_mul_i32 s56, s54, s29\n"
    "s_add_u32 m0, s60, 12288\n"
    "s_nop 0\n"
    "buffer_load_dwordx4 v21, s[8:11], s56 offen offset:0 lds\n"
    "buffer_load_dwordx4 v22, s[8:11], s56 offen offset:1024 lds\n"
    "buffer_load_dwordx4 v23, s[8:11], s56 offen offset:2048 lds\n"
    "s_add_u32 s31, s31, s30\n"
    "s_and_b32 s31, s31, 3\n"
    "s_and_b32 s49, s2, 7\n"
    "s_lshl_b32 s49, s49, 2\n"
    "s_add_u32 s31, s31, s49\n"
    "s_sub_u32 s49, s3, 1\n"
    "s_mul_i32 s49, s49, 19\n"
    "s_cmp_eq_u32 s3, 3\n"
    "s_cselect_b32 s50, 18, 19\n"
    "s_cmp_eq_u32 s3, 0\n"
    "s_cselect_b32 s50, 16, s50\n"
    "s_cselect_b32 s49, 0, s49\n"
    "s_cselect_b64 s[12:13], s[40:41], s[42:43]\n"
    "s_cselect_b32 s56, 0, 32\n"
    "s_mov_b64 s[20:21], s[40:41]\n"
    "s_mov_b64 s[26:27], s[42:43]\n"
    "s_lshl_b32 s51, s49, 15\n"
    "s_add_u32 s12, s12, s51\n"
    "s_addc_u32 s13, s13, 0\n"
    "s_lshl_b32 s14, s50, 15\n"
    "s_mov_b32 s15, 0x20000\n"
    "s_lshl_b32 s51, s52, 4\n"
    "s_add_u32 s51, s51, s63\n"
    "s_lshl_b32 s51, s51, 7\n"
    "s_add_u32 s51, s51, 42008576\n"
    "v_lshl_add_u32 v52, v0, 3, s51\n"
    "v_mov_b32 v53, 0x7fffff00\n"
    "v_cmp_gt_u32 vcc, 16, v0\n"
    "s_nop 1\n"
    "v_cndmask_b32 v15, v53, v52, vcc\n"
    "s_and_b32 s51, s52, 1\n"
    "s_lshl_b32 s51, s51, 3\n"
    "s_lshr_b32 s57, s52, 1\n"
    "s_add_u32 s51, s51, s57\n"
    "s_lshl_b32 s51, s51, 11\n"
    "s_add_u32 s51, s51, 41947136\n"
    "s_lshl_b32 s58, s63, 7\n"
    "s_add_u32 s58, s58, s51\n"
    "v_lshl_add_u32 v52, v0, 3, s58\n"
    "v_cndmask_b32 v127, v53, v52, vcc\n"
    "s_cmp_eq_u32 s57, 0\n"
    "s_cselect_b32 s58, 16, 8\n"
    "s_cmp_lg_u32 s48, 0\n"
    "s_cselect_b32 s58, 16, s58\n"
    "v_min_u32 v52, 143, v0\n"
    "v_max_u32 v54, s58, v52\n"
    "v_lshrrev_b32 v55, 3, v54\n"
    "v_and_b32 v54, 7, v54\n"
    "v_lshlrev_b32 v55, 7, v55\n"
    "v_lshl_add_u32 v55, v54, 3, v55\n"
    "s_sub_u32 s51, s51, 256\n"
    "v_add_u32 v34, s51, v55\n"
    "v_lshlrev_b32 v35, 2, v52\n"
    "s_lshl_b32 s49, s49, 1\n"
    "s_add_u32 s49, s49, s56\n"
    "s_and_b32 s51, s31, 1\n"
    "s_lshl_b32 s51, s51, 1\n"
    "s_lshr_b32 s56, s3, 1\n"
    "s_add_u32 s51, s51, s56\n"
    "s_mul_i32 s63, s51, 144\n"
    "s_add_u32 s63, s63, s49\n"
    "s_lshl_b32 s63, s63, 10\n"
    "s_xor_b32 s51, s51, 1\n"
    "s_mul_i32 s62, s51, 144\n"
    "s_add_u32 s62, s62, s49\n"
    "s_lshl_b32 s62, s62, 10\n"
    "s_lshl_b32 s50, s50, 11\n"
    "s_add_u32 s42, s62, s50\n"
    "s_add_u32 s46, s63, s50\n"
    "s_lshr_b32 s49, s31, 1\n"
    "s_mul_i32 s50, s49, 589824\n"
    "s_mul_i32 s64, s52, 589824\n"
    "s_add_u32 s40, s16, s50\n"
    "s_addc_u32 s41, s17, 0\n"
    "s_mov_b32 s43, 0x20000\n"
    "s_mov_b32 s47, 0x20000\n"
    "s_movk_i32 s65, 0xff\n"
    "s_cmp_eq_u32 s3, 1\n"
    "s_cselect_b32 s65, 0x0700, s65\n"
    "s_cmp_eq_u32 s3, 2\n"
    "s_cselect_b32 s65, 0x3c00, s65\n"
    "s_cmp_eq_u32 s3, 3\n"
    "s_cselect_b32 s65, 0xe000, s65\n"
    "s_lshl_b32 s50, s49, 11\n"
    "s_add_u32 s50, s50, 42008576\n"
    "v_lshl_add_u32 v52, v1, 7, s50\n"
    "s_mov_b32 vcc_lo, s65\n"
    "s_mov_b32 vcc_hi, 0\n"
    "v_cndmask_b32 v16, v53, v52, vcc\n"
    "v_lshlrev_b32 v14, 4, v1\n"
    "v_lshlrev_b32 v52, 1, v44\n"
    "v_xor_b32 v52, v43, v52\n"
    "v_lshlrev_b32 v52, 4, v52\n"
    "v_lshl_add_u32 v52, v42, 10, v52\n"
    "s_lshl_b32 s50, s30, 7\n"
    "v_add_u32 v2, s50, v52\n"
    "v_add_u32 v3, 7168, v2\n"
    "v_add_u32 v4, 7168, v3\n"
    "v_add_u32 v5, 7168, v4\n"
    "v_and_b32 v52, 3, v42\n"
    "v_lshlrev_b32 v52, 5, v52\n"
    "v_and_b32 v54, 3, v1\n"
    "v_lshl_add_u32 v52, v54, 3, v52\n"
    "v_bfe_u32 v54, v1, 2, 2\n"
    "v_lshl_add_u32 v52, v54, 7, v52\n"
    "v_lshl_add_u32 v54, v44, 9, v52\n"
    "v_add_u32 v6, s59, v54\n"
    "v_xor_b32 v7, 32, v6\n"
    "v_xor_b32 v8, 64, v6\n"
    "v_xor_b32 v9, 96, v6\n"
    "v_add_u32 v10, 98304, v6\n"
    "v_add_u32 v11, 98304, v7\n"
    "v_add_u32 v12, 98304, v8\n"
    "v_add_u32 v13, 98304, v9\n"
    "v_mul_u32_u24 v54, 3072, v44\n"
    "v_add_u32 v126, v54, v52\n"
    "v_and_b32 v54, 15, v1\n"
    "v_lshrrev_b32 v55, 1, v54\n"
    "v_xor_b32 v55, v44, v55\n"
    "v_lshlrev_b32 v55, 4, v55\n"
    "v_lshl_add_u32 v55, v54, 7, v55\n"
    "v_add_u32 v25, 49152, v55\n"
    "v_lshlrev_b32 v36, 3, v54\n"
    "v_lshlrev_b32 v37, 4, v44\n"
    "v_lshrrev_b32 v125, 5, v0\n"
    "v_lshlrev_b32 v125, 2, v125\n"
    "v_add_u32 v125, 113792, v125\n"
    "v_lshlrev_b32 v39, 2, v43\n"
    "v_add_u32 v39, 113792, v39\n"
    "v_add_u32 v36, 110592, v36\n"
    "v_add_u32 v37, 110592, v37\n"
    "s_waitcnt vmcnt(11)\n"
    "v_lshlrev_b32 v64, 16, v56\n"
    "s_add_u32 s55, s55, 1\n"
    "s_and_b32 s55, s55, 7\n"
    "s_lshl_b32 s57, s55, 7\n"
    "v_and_b32 v65, 0xffff0000, v56\n"
    "v_lshlrev_b32 v66, 16, v57\n"
    "v_and_b32 v67, 0xffff0000, v57\n"
    "s_add_u32 m0, s59, 69632\n"
    "v_lshlrev_b32 v68, 16, v58\n"
    "v_and_b32 v69, 0xffff0000, v58\n"
    "v_lshlrev_b32 v70, 16, v59\n"
    "buffer_load_dwordx4 v17, s[4:7], s57 offen offset:0 lds\n"
    "v_and_b32 v71, 0xffff0000, v59\n"
    "v_lshlrev_b32 v72, 16, v60\n"
    "v_and_b32 v73, 0xffff0000, v60\n"
    "v_lshlrev_b32 v74, 16, v61\n"
    "buffer_load_dwordx4 v18, s[4:7], s57 offen offset:1024 lds\n"
    "v_and_b32 v75, 0xffff0000, v61\n"
    "v_lshlrev_b32 v76, 16, v62\n"
    "v_and_b32 v77, 0xffff0000, v62\n"
    "buffer_load_dwordx4 v19, s[4:7], s57 offen offset:2048 lds\n"
    "v_lshlrev_b32 v78, 16, v63\n"
    "v_and_b32 v79, 0xffff0000, v63\n"
    "v_pk_add_f32 v[80:81], v[64:65], v[66:67]\n"
    "v_pk_add_f32 v[82:83], v[68:69], v[70:71]\n"
    "s_add_u32 m0, s59, 69632\n"
    "s_mul_i32 s58, s55, s28\n"
    "v_pk_add_f32 v[84:85], v[72:73], v[74:75]\n"
    "v_pk_add_f32 v[86:87], v[76:77], v[78:79]\n"
    "v_pk_add_f32 v[80:81], v[80:81], v[82:83]\n"
    "buffer_load_dwordx4 v20, s[4:7], s57 offen offset:3072 lds\n"
    "v_pk_add_f32 v[84:85], v[84:85], v[86:87]\n"
    "v_pk_add_f32 v[80:81], v[80:81], v[84:85]\n"
    "v_add_f32 v80, v80, v81\n"
    "s_nop 1\n"
    "v_add_f32_dpp v80, v80, v80 quad_perm:[1,0,3,2] row_mask:0xf bank_mask:0xf\n"
    "s_add_u32 m0, s61, 69632\n"
    "s_nop 0\n"
    "v_add_f32_dpp v80, v80, v80 quad_perm:[2,3,0,1] row_mask:0xf bank_mask:0xf\n"
    "s_nop 1\n"
    "v_add_f32_dpp v80, v80, v80 row_half_mirror row_mask:0xf bank_mask:0xf\n"
    "s_nop 1\n"
    "v_add_f32_dpp v80, v80, v80 row_mirror row_mask:0xf bank_mask:0xf\n"
    "buffer_load_dwordx4 v24, s[36:39], s58 offen offset:0 lds\n"
    "v_mov_b32 v81, v80\n"
    "s_nop 1\n"
    "v_permlane16_swap_b32 v80, v81\n"
    "v_add_f32 v80, v80, v81\n"
    "v_mul_f32 v80, 0x3b000000, v80\n"
    "s_add_u32 s54, s54, 1\n"
    "s_and_b32 s54, s54, 15\n"
    "s_mul_i32 s56, s54, s29\n"
    "v_pk_add_f32 v[64:65], v[64:65], v[80:81] op_sel_hi:[1,0] neg_lo:[0,1] neg_hi:[0,1]\n"
    "v_pk_add_f32 v[66:67], v[66:67], v[80:81] op_sel_hi:[1,0] neg_lo:[0,1] neg_hi:[0,1]\n"
    "v_pk_add_f32 v[68:69], v[68:69], v[80:81] op_sel_hi:[1,0] neg_lo:[0,1] neg_hi:[0,1]\n"
    "s_add_u32 m0, s60, 24576\n"
    "v_pk_add_f32 v[70:71], v[70:71], v[80:81] op_sel_hi:[1,0] neg_lo:[0,1] neg_hi:[0,1]\n"
    "v_pk_add_f32 v[72:73], v[72:73], v[80:81] op_sel_hi:[1,0] neg_lo:[0,1] neg_hi:[0,1]\n"
    "v_pk_add_f32 v[74:75], v[74:75], v[80:81] op_sel_hi:[1,0] neg_lo:[0,1] neg_hi:[0,1]\n"
    "v_pk_add_f32 v[76:77], v[76:77], v[80:81] op_sel_hi:[1,0] neg_lo:[0,1] neg_hi:[0,1]\n"
    "buffer_load_dwordx4 v21, s[8:11], s56 offen offset:0 lds\n"
    "v_pk_add_f32 v[78:79], v[78:79], v[80:81] op_sel_hi:[1,0] neg_lo:[0,1] neg_hi:[0,1]\n"
    "v_pk_mul_f32 v[82:83], v[64:65], v[64:65]\n"
    "v_pk_mul_f32 v[84:85], v[66:67], v[66:67]\n"
    "buffer_load_dwordx4 v22, s[8:11], s56 offen offset:1024 lds\n"
    "v_pk_fma_f32 v[82:83], v[68:69], v[68:69], v[82:83]\n"
    "v_pk_fma_f32 v[84:85], v[70:71], v[70:71], v[84:85]\n"
    "v_pk_fma_f32 v[82:83], v[72:73], v[72:73], v[82:83]\n"
    "v_pk_fma_f32 v[84:85], v[74:75], v[74:75], v[84:85]\n"
    "buffer_load_dwordx4 v23, s[8:11], s56 offen offset:2048 lds\n"
    "v_pk_fma_f32 v[82:83], v[76:77], v[76:77], v[82:83]\n"
    "v_pk_fma_f32 v[84:85], v[78:79], v[78:79], v[84:85]\n"
    "v_pk_add_f32 v[82:83], v[82:83], v[84:85]\n"
    "s_add_u32 s54, s54, 1\n"
    "s_and_b32 s54, s54, 15\n"
    "s_mul_i32 s56, s54, s29\n"
    "v_add_f32 v82, v82, v83\n"
    "s_nop 1\n"
    "v_add_f32_dpp v82, v82, v82 quad_perm:[1,0,3,2] row_mask:0xf bank_mask:0xf\n"
    "s_nop 1\n"
    "v_add_f32_dpp v82, v82, v82 quad_perm:[2,3,0,1] row_mask:0xf bank_mask:0xf\n"
    "s_nop 1\n"
    "v_add_f32_dpp v82, v82, v82 row_half_mirror row_mask:0xf bank_mask:0xf\n"
    "s_add_u32 m0, s60, 36864\n"
    "s_nop 0\n"
    "v_add_f32_dpp v82, v82, v82 row_mirror row_mask:0xf bank_mask:0xf\n"
    "v_mov_b32 v83, v82\n"
    "s_nop 1\n"
    "v_permlane16_swap_b32 v82, v83\n"
    "buffer_load_dwordx4 v21, s[8:11], s56 offen offset:0 lds\n"
    "v_add_f32 v82, v82, v83\n"
    "v_mul_f32 v82, 0x3b000000, v82\n"
    "v_cvt_pk_bf16_f32 v82, v82, v82\n"
    "v_lshlrev_b32 v82, 16, v82\n"
    "buffer_load_dwordx4 v22, s[8:11], s56 offen offset:1024 lds\n"
    "v_add_f32 v82, 0x3727c5ac, v82\n"
    "v_cvt_pk_bf16_f32 v82, v82, v82\n"
    "v_lshlrev_b32 v82, 16, v82\n"
    "buffer_load_dwordx4 v23, s[8:11], s56 offen offset:2048 lds\n"
    "v_sqrt_f32 v82, v82\n"
    "s_nop 0\n"
    "v_cvt_pk_bf16_f32 v82, v80, v82\n"
    "ds_write_b32 v125, v82\n"
    "s_waitcnt lgkmcnt(0)\n"
    "s_add_u32 s66, s66, 0x7f4a7c15\n"
    "s_addc_u32 s67, s67, 0x9e3779b9\n"
    "s_mov_b64 s[44:45], s[40:41]\n"
    "s_cmp_lg_u32 s48, 0\n"
    "s_cbranch_scc1 L_ffn\n"
    "s_lshl_b32 s50, s3, 5\n"
    "v_xor_b32 v27, s50, v126\n"
    "s_min_u32 s50, s3, 2\n"
    "s_mul_i32 s50, s50, 20480\n"
    "s_add_u32 s50, s50, 65536\n"
    "v_add_u32 v40, s50, v14\n"
    "v_mov_b32 v120, 0x3f803f80\n"
    "v_mov_b32 v121, 0x3f803f80\n"
    "v_mov_b32 v122, 0x3f803f80\n"
    "v_mov_b32 v123, 0x3f803f80\n"
    "ds_write_b128 v40, v[120:123]\n"
    "s_nop 0\n"
    "v_lshrrev_b32 v40, 4, v1\n"
    "v_and_b32 v41, 15, v1\n"
    "v_lshlrev_b32 v40, 2, v40\n"
    "v_sub_u32 v40, v40, v41\n"
    "v_mov_b32 v42, 0xff800000\n"
    "v_mov_b32 v43, 0xffff\n"
    "v_mov_b32 v44, 0xffff0000\n"
    "v_add_u32 v41, 0, v40\n"
    "v_cmp_lt_i32 vcc, 8, v41\n"
    "s_nop 1\n"
    "v_cndmask_b32 v112, v42, 0, vcc\n"
    "v_cndmask_b32 v45, 0, v43, vcc\n"
    "v_add_u32 v41, 7, v41\n"
    "v_cmp_gt_u32 vcc, 8, v41\n"
    "s_nop 1\n"
    "v_cndmask_b32 v116, v42, 0, vcc\n"
    "v_add_u32 v41, 1, v40\n"
    "v_cmp_lt_i32 vcc, 8, v41\n"
    "s_nop 1\n"
    "v_cndmask_b32 v113, v42, 0, vcc\n"
    "v_cndmask_b32 v46, 0, v44, vcc\n"
    "v_add_u32 v41, 7, v41\n"
    "v_cmp_gt_u32 vcc, 8, v41\n"
    "s_nop 1\n"
    "v_cndmask_b32 v117, v42, 0, vcc\n"
    "v_or_b32 v124, v45, v46\n"
    "v_add_u32 v41, 2, v40\n"
    "v_cmp_lt_i32 vcc, 8, v41\n"
    "s_nop 1\n"
    "v_cndmask_b32 v114, v42, 0, vcc\n"
    "v_cndmask_b32 v45, 0, v43, vcc\n"
    "v_add_u32 v41, 7, v41\n"
    "v_cmp_gt_u32 vcc, 8, v41\n"
    "s_nop 1\n"
    "v_cndmask_b32 v118, v42, 0, vcc\n"
    "v_add_u32 v41, 3, v40\n"
    "v_cmp_lt_i32 vcc, 8, v41\n"
    "s_nop 1\n"
    "v_cndmask_b32 v115, v42, 0, vcc\n"
    "v_cndmask_b32 v46, 0, v44, vcc\n"
    "v_add_u32 v41, 7, v41\n"
    "v_cmp_gt_u32 vcc, 8, v41\n"
    "s_nop 1\n"
    "v_cndmask_b32 v119, v42, 0, vcc\n"
    "v_or_b32 v125, v45, v46\n"
    "s_mul_i32 s50, s2, 144\n"
    "s_mul_i32 s51, s3, 36\n"
    "s_add_u32 s50, s50, s51\n"
    "v_add_lshl_u32 v41, v1, s50, 7\n"
    "v_mov_b32 v42, 0x7fffff00\n"
    "v_cmp_gt_u32 vcc, 36, v1\n"
    "s_nop 1\n"
    "v_cndmask_b32 v28, v42, v41, vcc\n"
    "v_subrev_u32 v29, 524288, v28\n"
    "s_mov_b32 s22, 524288\n"
    "s_mov_b64 s[32:33], s[26:27]\n"
    "v_xor_b32 v26, 64, v25\n"
    "s_waitcnt vmcnt(14) lgkmcnt(0)\n"
    "s_barrier\n"
    "ds_read_b64_tr_b16 a[196:197], v27 offset:0\n"
    "ds_read_b64_tr_b16 a[198:199], v27 offset:512\n"
    "ds_read_b64_tr_b16 a[200:201], v27 offset:1024\n"
    "ds_read_b64_tr_b16 a[202:203], v27 offset:1536\n"
    "ds_read_b64_tr_b16 a[204:205], v27 offset:2048\n"
    "ds_read_b64_tr_b16 a[206:207], v27 offset:2560\n"
    "ds_read_b128 a[160:163], v25 offset:16384\n"
    "ds_read_b128 a[164:167], v25 offset:0\n"
    "ds_read_b128 a[168:171], v25 offset:2048\n"
    "ds_read_b128 a[172:175], v25 offset:4096\n"
    "ds_read_b128 a[176:179], v25 offset:6144\n"
    "ds_read_b128 a[180:183], v25 offset:8192\n"
    "ds_read_b128 a[184:187], v25 offset:10240\n"
    "ds_read_b128 a[188:191], v25 offset:12288\n"
    "ds_read_b128 a[192:195], v25 offset:14336\n"
    "s_waitcnt lgkmcnt(0)\n"
    "v_mfma_f32_16x16x32_bf16 v[128:131], a[196:199], a[160:163], 0\n"
    "s_waitcnt vmcnt(11)\n"
    "s_barrier\n"
    "ds_read_b64_tr_b16 a[244:245], v27 offset:12288\n"
    "v_mfma_f32_16x16x32_bf16 v[164:167], a[200:203], a[160:163], 0\n"
    "ds_read_b64_tr_b16 a[246:247], v27 offset:12800\n"
    "s_add_u32 s55, s55, 1\n"
    "s_and_b32 s55, s55, 7\n"
    "s_lshl_b32 s57, s55, 7\n"
    "v_mfma_f32_16x16x32_bf16 v[200:203], a[160:163], a[204:207], 0\n"
    "ds_read_b64_tr_b16 a[248:249], v27 offset:13312\n"
    "s_add_u32 m0, s59, 90112\n"
    "v_mfma_f32_16x16x32_bf16 v[132:135], a[196:199], a[164:167], 0\n"
    "ds_read_b64_tr_b16 a[250:251], v27 offset:13824\n"
    "v_mfma_f32_16x16x32_bf16 v[168:171], a[200:203], a[164:167], 0\n"
    "buffer_load_dwordx4 v17, s[4:7], s57 offen offset:0 lds\n"
    "ds_read_b64_tr_b16 a[252:253], v27 offset:14336\n"
    "v_mfma_f32_16x16x32_bf16 v[204:207], a[164:167], a[204:207], 0\n"
    "buffer_load_dwordx4 v18, s[4:7], s57 offen offset:1024 lds\n"
    "ds_read_b64_tr_b16 a[254:255], v27 offset:14848\n"
    "v_mfma_f32_16x16x32_bf16 v[136:139], a[196:199], a[168:171], 0\n"
    "ds_read_b128 a[208:211], v26 offset:16384\n"
    "buffer_load_dwordx4 v19, s[4:7], s57 offen offset:2048 lds\n"
    "v_mfma_f32_16x16x32_bf16 v[172:175], a[200:203], a[168:171], 0\n"
    "ds_read_b128 a[212:215], v26 offset:0\n"
    "v_mfma_f32_16x16x32_bf16 v[208:211], a[168:171], a[204:207], 0\n"
    "s_add_u32 m0, s59, 90112\n"
    "s_mul_i32 s58, s55, s28\n"
    "ds_read_b128 a[216:219], v26 offset:2048\n"
    "v_mfma_f32_16x16x32_bf16 v[140:143], a[196:199], a[172:175], 0\n"
    "buffer_load_dwordx4 v20, s[4:7], s57 offen offset:3072 lds\n"
    "ds_read_b128 a[220:223], v26 offset:4096\n"
    "v_mfma_f32_16x16x32_bf16 v[176:179], a[200:203], a[172:175], 0\n"
    "ds_read_b128 a[224:227], v26 offset:6144\n"
    "s_add_u32 m0, s61, 90112\n"
    "v_mfma_f32_16x16x32_bf16 v[212:215], a[172:175], a[204:207], 0\n"
    "ds_read_b128 a[228:231], v26 offset:8192\n"
    "v_mfma_f32_16x16x32_bf16 v[144:147], a[196:199], a[176:179], 0\n"
    "buffer_load_dwordx4 v24, s[36:39], s58 offen offset:0 lds\n"
    "ds_read_b128 a[232:235], v26 offset:10240\n"
    "v_mfma_f32_16x16x32_bf16 v[180:183], a[200:203], a[176:179], 0\n"
    "s_add_u32 s54, s54, 1\n"
    "s_and_b32 s54, s54, 15\n"
    "s_mul_i32 s56, s54, s29\n"
    "ds_read_b128 a[236:239], v26 offset:12288\n"
    "v_mfma_f32_16x16x32_bf16 v[216:219], a[176:179], a[204:207], 0\n"
    "ds_read_b128 a[240:243], v26 offset:14336\n"
    "s_add_u32 m0, s60, 0\n"
    "v_mfma_f32_16x16x32_bf16 v[148:151], a[196:199], a[180:183], 0\n"
    "ds_read_b32 v110, v39\n"
    "v_mfma_f32_16x16x32_bf16 v[184:187], a[200:203], a[180:183], 0\n"
    "buffer_load_dwordx4 v21, s[8:11], s56 offen offset:0 lds\n"
    "v_mfma_f32_16x16x32_bf16 v[220:223], a[180:183], a[204:207], 0\n"
    "v_mfma_f32_16x16x32_bf16 v[152:155], a[196:199], a[184:187], 0\n"
    "buffer_load_dwordx4 v22, s[8:11], s56 offen offset:1024 lds\n"
    "v_mfma_f32_16x16x32_bf16 v[188:191], a[200:203], a[184:187], 0\n"
    "v_mfma_f32_16x16x32_bf16 v[224:227], a[184:187], a[204:207], 0\n"
    "v_mfma_f32_16x16x32_bf16 v[156:159], a[196:199], a[188:191], 0\n"
    "buffer_load_dwordx4 v23, s[8:11], s56 offen offset:2048 lds\n"
    "v_mfma_f32_16x16x32_bf16 v[192:195], a[200:203], a[188:191], 0\n"
    "v_mfma_f32_16x16x32_bf16 v[228:231], a[188:191], a[204:207], 0\n"
    "v_mfma_f32_16x16x32_bf16 v[160:163], a[196:199], a[192:195], 0\n"
    "v_mfma_f32_16x16x32_bf16 v[196:199], a[200:203], a[192:195], 0\n"
    "v_mfma_f32_16x16x32_bf16 v[232:235], a[192:195], a[204:207], 0\n"
    "s_waitcnt lgkmcnt(0)\n"
    "v_mfma_f32_16x16x32_bf16 v[128:131], a[244:247], a[208:211], v[128:131]\n"
    "s_waitcnt vmcnt(11)\n"
    "s_barrier\n"
    "ds_read_b64_tr_b16 a[196:197], v27 offset:24576\n"
    "v_mfma_f32_16x16x32_bf16 v[164:167], a[248:251], a[208:211], v[164:167]\n"
    "ds_read_b64_tr_b16 a[198:199], v27 offset:25088\n"
    "s_add_u32 s54, s54, 1\n"
    "s_and_b32 s54, s54, 15\n"
    "s_mul_i32 s56, s54, s29\n"
    "v_mfma_f32_16x16x32_bf16 v[200:203], a[208:211], a[252:255], v[200:203]\n"
    "ds_read_b64_tr_b16 a[200:201], v27 offset:25600\n"
    "v_mfma_f32_16x16x32_bf16 v[132:135], a[244:247], a[212:215], v[132:135]\n"
    "s_add_u32 m0, s60, 12288\n"
    "ds_read_b64_tr_b16 a[202:203], v27 offset:26112\n"
    "v_mfma_f32_16x16x32_bf16 v[168:171], a[248:251], a[212:215], v[168:171]\n"
    "ds_read_b64_tr_b16 a[204:205], v27 offset:26624\n"
    "v_mfma_f32_16x16x32_bf16 v[204:207], a[212:215], a[252:255], v[204:207]\n"
    "buffer_load_dwordx4 v21, s[8:11], s56 offen offset:0 lds\n"
    "ds_read_b64_tr_b16 a[206:207], v27 offset:27136\n"
    "v_mfma_f32_16x16x32_bf16 v[136:139], a[244:247], a[216:219], v[136:139]\n"
    "ds_read_b128 a[160:163], v25 offset:36864\n"
    "v_mfma_f32_16x16x32_bf16 v[172:175], a[248:251], a[216:219], v[172:175]\n"
    "buffer_load_dwordx4 v22, s[8:11], s56 offen offset:1024 lds\n"
    "v_mov_b32 v111, s66\n"
    "ds_read_b128 a[164:167], v25 offset:20480\n"
    "v_mfma_f32_16x16x32_bf16 v[208:211], a[216:219], a[252:255], v[208:211]\n"
    "ds_read_b128 a[168:171], v25 offset:22528\n"
    "buffer_load_dwordx4 v23, s[8:11], s56 offen offset:2048 lds\n"
    "v_mfma_f32_16x16x32_bf16 v[140:143], a[244:247], a[220:223], v[140:143]\n"
    "ds_read_b128 a[172:175], v25 offset:24576\n"
    "v_mfma_f32_16x16x32_bf16 v[176:179], a[248:251], a[220:223], v[176:179]\n"
    "ds_read_b128 a[176:179], v25 offset:26624\n"
    "s_add_u32 s55, s55, 1\n"
    "s_and_b32 s55, s55, 7\n"
    "s_lshl_b32 s57, s55, 7\n"
    "v_mfma_f32_16x16x32_bf16 v[212:215], a[220:223], a[252:255], v[212:215]\n"
    "ds_read_b128 a[180:183], v25 offset:28672\n"
    "v_mfma_f32_16x16x32_bf16 v[144:147], a[244:247], a[224:227], v[144:147]\n"
    "ds_read_b128 a[184:187], v25 offset:30720\n"
    "s_add_u32 m0, s59, 49152\n"
    "v_mfma_f32_16x16x32_bf16 v[180:183], a[248:251], a[224:227], v[180:183]\n"
    "ds_read_b128 a[188:191], v25 offset:32768\n"
    "buffer_store_dwordx2 v[110:111], v127, s[16:19], 0 offen sc1\n"
    "v_mfma_f32_16x16x32_bf16 v[216:219], a[224:227], a[252:255], v[216:219]\n"
    "buffer_load_dwordx4 v17, s[4:7], s57 offen offset:0 lds\n"
    "ds_read_b128 a[192:195], v25 offset:34816\n"
    "v_mfma_f32_16x16x32_bf16 v[148:151], a[244:247], a[228:231], v[148:151]\n"
    "v_mfma_f32_16x16x32_bf16 v[184:187], a[248:251], a[228:231], v[184:187]\n"
    "v_mfma_f32_16x16x32_bf16 v[220:223], a[228:231], a[252:255], v[220:223]\n"
    "buffer_load_dwordx4 v18, s[4:7], s57 offen offset:1024 lds\n"
    "v_mfma_f32_16x16x32_bf16 v[152:155], a[244:247], a[232:235], v[152:155]\n"
    "v_mfma_f32_16x16x32_bf16 v[188:191], a[248:251], a[232:235], v[188:191]\n"
    "v_mfma_f32_16x16x32_bf16 v[224:227], a[232:235], a[252:255], v[224:227]\n"
    "v_mfma_f32_16x16x32_bf16 v[156:159], a[244:247], a[236:239], v[156:159]\n"
    "buffer_load_dwordx4 v19, s[4:7], s57 offen offset:2048 lds\n"
    "v_mfma_f32_16x16x32_bf16 v[192:195], a[248:251], a[236:239], v[192:195]\n"
    "v_mfma_f32_16x16x32_bf16 v[228:231], a[236:239], a[252:255], v[228:231]\n"
    "v_mfma_f32_16x16x32_bf16 v[160:163], a[244:247], a[240:243], v[160:163]\n"
    "v_mfma_f32_16x16x32_bf16 v[196:199], a[248:251], a[240:243], v[196:199]\n"
    "v_mfma_f32_16x16x32_bf16 v[232:235], a[240:243], a[252:255], v[232:235]\n"
    "s_waitcnt lgkmcnt(0)\n"
    "v_mfma_f32_16x16x32_bf16 v[128:131], a[196:199], a[160:163], v[128:131]\n"
    "s_waitcnt vmcnt(15)\n"
    "s_barrier\n"
    "ds_read_b64_tr_b16 a[244:245], v27 offset:36864\n"
    "v_mfma_f32_16x16x32_bf16 v[164:167], a[200:203], a[160:163], v[164:167]\n"
    "ds_read_b64_tr_b16 a[246:247], v27 offset:37376\n"
    "s_add_u32 m0, s59, 49152\n"
    "s_mul_i32 s58, s55, s28\n"
    "v_mfma_f32_16x16x32_bf16 v[200:203], a[160:163], a[204:207], v[200:203]\n"
    "ds_read_b64_tr_b16 a[248:249], v27 offset:37888\n"
    "v_mfma_f32_16x16x32_bf16 v[132:135], a[196:199], a[164:167], v[132:135]\n"
    "ds_read_b64_tr_b16 a[250:251], v27 offset:38400\n"
    "buffer_load_dwordx4 v20, s[4:7], s57 offen offset:3072 lds\n"
    "v_mfma_f32_16x16x32_bf16 v[168:171], a[200:203], a[164:167], v[168:171]\n"
    "ds_read_b64_tr_b16 a[252:253], v27 offset:38912\n"
    "v_mfma_f32_16x16x32_bf16 v[204:207], a[164:167], a[204:207], v[204:207]\n"
    "ds_read_b64_tr_b16 a[254:255], v27 offset:39424\n"
    "s_add_u32 m0, s61, 49152\n"
    "v_mfma_f32_16x16x32_bf16 v[136:139], a[196:199], a[168:171], v[136:139]\n"
    "ds_read_b128 a[208:211], v26 offset:36864\n"
    "v_mfma_f32_16x16x32_bf16 v[172:175], a[200:203], a[168:171], v[172:175]\n"
    "ds_read_b128 a[212:215], v26 offset:20480\n"
    "buffer_load_dwordx4 v24, s[36:39], s58 offen offset:0 lds\n"
    "v_mfma_f32_16x16x32_bf16 v[208:211], a[168:171], a[204:207], v[208:211]\n"
    "ds_read_b128 a[216:219], v26 offset:22528\n"
    "v_mfma_f32_16x16x32_bf16 v[140:143], a[196:199], a[172:175], v[140:143]\n"
    "ds_read_b128 a[220:223], v26 offset:24576\n"
    "s_add_u32 s54, s54, 1\n"
    "s_and_b32 s54, s54, 15\n"
    "s_mul_i32 s56, s54, s29\n"
    "v_mfma_f32_16x16x32_bf16 v[176:179], a[200:203], a[172:175], v[176:179]\n"
    "ds_read_b128 a[224:227], v26 offset:26624\n"
    "v_mfma_f32_16x16x32_bf16 v[212:215], a[172:175], a[204:207], v[212:215]\n"
    "ds_read_b128 a[228:231], v26 offset:28672\n"
    "s_add_u32 m0, s60, 24576\n"
    "v_mfma_f32_16x16x32_bf16 v[144:147], a[196:199], a[176:179], v[144:147]\n"
    "ds_read_b128 a[232:235], v26 offset:30720\n"
    "v_mfma_f32_16x16x32_bf16 v[180:183], a[200:203], a[176:179], v[180:183]\n"
    "ds_read_b128 a[236:239], v26 offset:32768\n"
    "buffer_load_dwordx4 v21, s[8:11], s56 offen offset:0 lds\n"
    "v_mfma_f32_16x16x32_bf16 v[216:219], a[176:179], a[204:207], v[216:219]\n"
    "ds_read_b128 a[240:243], v26 offset:34816\n"
    "v_mfma_f32_16x16x32_bf16 v[148:151], a[196:199], a[180:183], v[148:151]\n"
    "v_mfma_f32_16x16x32_bf16 v[184:187], a[200:203], a[180:183], v[184:187]\n"
    "buffer_load_dwordx4 v22, s[8:11], s56 offen offset:1024 lds\n"
    "v_mfma_f32_16x16x32_bf16 v[220:223], a[180:183], a[204:207], v[220:223]\n"
    "v_mfma_f32_16x16x32_bf16 v[152:155], a[196:199], a[184:187], v[152:155]\n"
    "v_mfma_f32_16x16x32_bf16 v[188:191], a[200:203], a[184:187], v[188:191]\n"
    "v_mfma_f32_16x16x32_bf16 v[224:227], a[184:187], a[204:207], v[224:227]\n"
    "buffer_load_dwordx4 v23, s[8:11], s56 offen offset:2048 lds\n"
    "v_mfma_f32_16x16x32_bf16 v[156:159], a[196:199], a[188:191], v[156:159]\n"
    "v_mfma_f32_16x16x32_bf16 v[192:195], a[200:203], a[188:191], v[192:195]\n"
    "v_mfma_f32_16x16x32_bf16 v[228:231], a[188:191], a[204:207], v[228:231]\n"
    "v_mfma_f32_16x16x32_bf16 v[160:163], a[196:199], a[192:195], v[160:163]\n"
    "v_mfma_f32_16x16x32_bf16 v[196:199], a[200:203], a[192:195], v[196:199]\n"
    "v_mfma_f32_16x16x32_bf16 v[232:235], a[192:195], a[204:207], v[232:235]\n"
    "s_waitcnt lgkmcnt(0)\n"
    "v_mfma_f32_16x16x32_bf16 v[128:131], a[244:247], a[208:211], v[128:131]\n"
    "s_waitcnt vmcnt(12)\n"
    "s_barrier\n"
    "ds_read_b64_tr_b16 a[196:197], v27 offset:0\n"
    "v_mfma_f32_16x16x32_bf16 v[164:167], a[248:251], a[208:211], v[164:167]\n"
    "ds_read_b64_tr_b16 a[198:199], v27 offset:512\n"
    "s_add_u32 s54, s54, 1\n"
    "s_and_b32 s54, s54, 15\n"
    "s_mul_i32 s56, s54, s29\n"
    "v_mfma_f32_16x16x32_bf16 v[200:203], a[208:211], a[252:255], v[200:203]\n"
    "ds_read_b64_tr_b16 a[200:201], v27 offset:1024\n"
    "v_mfma_f32_16x16x32_bf16 v[132:135], a[244:247], a[212:215], v[132:135]\n"
    "s_add_u32 m0, s60, 36864\n"
    "ds_read_b64_tr_b16 a[202:203], v27 offset:1536\n"
    "v_mfma_f32_16x16x32_bf16 v[168:171], a[248:251], a[212:215], v[168:171]\n"
    "ds_read_b64_tr_b16 a[204:205], v27 offset:2048\n"
    "v_mfma_f32_16x16x32_bf16 v[204:207], a[212:215], a[252:255], v[204:207]\n"
    "buffer_load_dwordx4 v21, s[8:11], s56 offen offset:0 lds\n"
    "ds_read_b64_tr_b16 a[206:207], v27 offset:2560\n"
    "v_mfma_f32_16x16x32_bf16 v[136:139], a[244:247], a[216:219], v[136:139]\n"
    "ds_read_b128 a[160:163], v25 offset:57344\n"
    "v_mfma_f32_16x16x32_bf16 v[172:175], a[248:251], a[216:219], v[172:175]\n"
    "buffer_load_dwordx4 v22, s[8:11], s56 offen offset:1024 lds\n"
    "ds_read_b128 a[164:167], v25 offset:40960\n"
    "v_mfma_f32_16x16x32_bf16 v[208:211], a[216:219], a[252:255], v[208:211]\n"
    "ds_read_b128 a[168:171], v25 offset:43008\n"
    "buffer_load_dwordx4 v23, s[8:11], s56 offen offset:2048 lds\n"
    "v_mfma_f32_16x16x32_bf16 v[140:143], a[244:247], a[220:223], v[140:143]\n"
    "ds_read_b128 a[172:175], v25 offset:45056\n"
    "v_mfma_f32_16x16x32_bf16 v[176:179], a[248:251], a[220:223], v[176:179]\n"
    "ds_read_b128 a[176:179], v25 offset:47104\n"
    "s_add_u32 s55, s55, 1\n"
    "s_and_b32 s55, s55, 7\n"
    "s_lshl_b32 s57, s55, 7\n"
    "v_mfma_f32_16x16x32_bf16 v[212:215], a[220:223], a[252:255], v[212:215]\n"
    "ds_read_b128 a[180:183], v25 offset:49152\n"
    "v_mfma_f32_16x16x32_bf16 v[144:147], a[244:247], a[224:227], v[144:147]\n"
    "ds_read_b128 a[184:187], v25 offset:51200\n"
    "s_add_u32 m0, s59, 69632\n"
    "v_mfma_f32_16x16x32_bf16 v[180:183], a[248:251], a[224:227], v[180:183]\n"
    "ds_read_b128 a[188:191], v25 offset:53248\n"
    "v_mfma_f32_16x16x32_bf16 v[216:219], a[224:227], a[252:255], v[216:219]\n"
    "buffer_load_dwordx4 v17, s[4:7], s57 offen offset:0 lds\n"
    "ds_read_b128 a[192:195], v25 offset:55296\n"
    "v_mfma_f32_16x16x32_bf16 v[148:151], a[244:247], a[228:231], v[148:151]\n"
    "v_mfma_f32_16x16x32_bf16 v[184:187], a[248:251], a[228:231], v[184:187]\n"
    "v_mfma_f32_16x16x32_bf16 v[220:223], a[228:231], a[252:255], v[220:223]\n"
    "buffer_load_dwordx4 v18, s[4:7], s57 offen offset:1024 lds\n"
    "v_mfma_f32_16x16x32_bf16 v[152:155], a[244:247], a[232:235], v[152:155]\n"
    "v_mfma_f32_16x16x32_bf16 v[188:191], a[248:251], a[232:235], v[188:191]\n"
    "v_mfma_f32_16x16x32_bf16 v[224:227], a[232:235], a[252:255], v[224:227]\n"
    "v_mfma_f32_16x16x32_bf16 v[156:159], a[244:247], a[236:239], v[156:159]\n"
    "buffer_load_dwordx4 v19, s[4:7], s57 offen offset:2048 lds\n"
    "v_mfma_f32_16x16x32_bf16 v[192:195], a[248:251], a[236:239], v[192:195]\n"
    "v_mfma_f32_16x16x32_bf16 v[228:231], a[236:239], a[252:255], v[228:231]\n"
    "v_mfma_f32_16x16x32_bf16 v[160:163], a[244:247], a[240:243], v[160:163]\n"
    "v_mfma_f32_16x16x32_bf16 v[196:199], a[248:251], a[240:243], v[196:199]\n"
    "v_mfma_f32_16x16x32_bf16 v[232:235], a[240:243], a[252:255], v[232:235]\n"
    "s_waitcnt lgkmcnt(0)\n"
    "v_mfma_f32_16x16x32_bf16 v[128:131], a[196:199], a[160:163], v[128:131]\n"
    "s_waitcnt vmcnt(15)\n"
    "s_barrier\n"
    "ds_read_b64_tr_b16 a[244:245], v27 offset:12288\n"
    "v_mfma_f32_16x16x32_bf16 v[164:167], a[200:203], a[160:163], v[164:167]\n"
    "ds_read_b64_tr_b16 a[246:247], v27 offset:12800\n"
    "s_add_u32 m0, s59, 69632\n"
    "s_mul_i32 s58, s55, s28\n"
    "v_mfma_f32_16x16x32_bf16 v[200:203], a[160:163], a[204:207], v[200:203]\n"
    "ds_read_b64_tr_b16 a[248:249], v27 offset:13312\n"
    "v_mfma_f32_16x16x32_bf16 v[132:135], a[196:199], a[164:167], v[132:135]\n"
    "ds_read_b64_tr_b16 a[250:251], v27 offset:13824\n"
    "buffer_load_dwordx4 v20, s[4:7], s57 offen offset:3072 lds\n"
    "v_mfma_f32_16x16x32_bf16 v[168:171], a[200:203], a[164:167], v[168:171]\n"
    "ds_read_b64_tr_b16 a[252:253], v27 offset:14336\n"
    "v_mfma_f32_16x16x32_bf16 v[204:207], a[164:167], a[204:207], v[204:207]\n"
    "ds_read_b64_tr_b16 a[254:255], v27 offset:14848\n"
    "s_add_u32 m0, s61, 69632\n"
    "v_mfma_f32_16x16x32_bf16 v[136:139], a[196:199], a[168:171], v[136:139]\n"
    "ds_read_b128 a[208:211], v26 offset:57344\n"
    "v_mfma_f32_16x16x32_bf16 v[172:175], a[200:203], a[168:171], v[172:175]\n"
    "ds_read_b128 a[212:215], v26 offset:40960\n"
    "buffer_load_dwordx4 v24, s[36:39], s58 offen offset:0 lds\n"
    "v_mfma_f32_16x16x32_bf16 v[208:211], a[168:171], a[204:207], v[208:211]\n"
    "ds_read_b128 a[216:219], v26 offset:43008\n"
    "v_mfma_f32_16x16x32_bf16 v[140:143], a[196:199], a[172:175], v[140:143]\n"
    "ds_read_b128 a[220:223], v26 offset:45056\n"
    "s_add_u32 s54, s54, 1\n"
    "s_and_b32 s54, s54, 15\n"
    "s_mul_i32 s56, s54, s29\n"
    "v_mfma_f32_16x16x32_bf16 v[176:179], a[200:203], a[172:175], v[176:179]\n"
    "ds_read_b128 a[224:227], v26 offset:47104\n"
    "v_mfma_f32_16x16x32_bf16 v[212:215], a[172:175], a[204:207], v[212:215]\n"
    "ds_read_b128 a[228:231], v26 offset:49152\n"
    "s_add_u32 m0, s60, 0\n"
    "v_mfma_f32_16x16x32_bf16 v[144:147], a[196:199], a[176:179], v[144:147]\n"
    "ds_read_b128 a[232:235], v26 offset:51200\n"
    "v_mfma_f32_16x16x32_bf16 v[180:183], a[200:203], a[176:179], v[180:183]\n"
    "ds_read_b128 a[236:239], v26 offset:53248\n"
    "buffer_load_dwordx4 v21, s[8:11], s56 offen offset:0 lds\n"
    "v_mfma_f32_16x16x32_bf16 v[216:219], a[176:179], a[204:207], v[216:219]\n"
    "ds_read_b128 a[240:243], v26 offset:55296\n"
    "v_mfma_f32_16x16x32_bf16 v[148:151], a[196:199], a[180:183], v[148:151]\n"
    "v_mfma_f32_16x16x32_bf16 v[184:187], a[200:203], a[180:183], v[184:187]\n"
    "buffer_load_dwordx4 v22, s[8:11], s56 offen offset:1024 lds\n"
    "v_mfma_f32_16x16x32_bf16 v[220:223], a[180:183], a[204:207], v[220:223]\n"
    "v_mfma_f32_16x16x32_bf16 v[152:155], a[196:199], a[184:187], v[152:155]\n"
    "v_mfma_f32_16x16x32_bf16 v[188:191], a[200:203], a[184:187], v[188:191]\n"
    "v_mfma_f32_16x16x32_bf16 v[224:227], a[184:187], a[204:207], v[224:227]\n"
    "buffer_load_dwordx4 v23, s[8:11], s56 offen offset:2048 lds\n"
    "v_mfma_f32_16x16x32_bf16 v[156:159], a[196:199], a[188:191], v[156:159]\n"
    "v_mfma_f32_16x16x32_bf16 v[192:195], a[200:203], a[188:191], v[192:195]\n"
    "v_mfma_f32_16x16x32_bf16 v[228:231], a[188:191], a[204:207], v[228:231]\n"
    "v_mfma_f32_16x16x32_bf16 v[160:163], a[196:199], a[192:195], v[160:163]\n"
    "v_mfma_f32_16x16x32_bf16 v[196:199], a[200:203], a[192:195], v[196:199]\n"
    "v_mfma_f32_16x16x32_bf16 v[232:235], a[192:195], a[204:207], v[232:235]\n"
    "s_waitcnt lgkmcnt(0)\n"
    "v_mfma_f32_16x16x32_bf16 v[128:131], a[244:247], a[208:211], v[128:131]\n"
    "s_waitcnt vmcnt(11)\n"
    "s_barrier\n"
    "ds_read_b64_tr_b16 a[196:197], v27 offset:24576\n"
    "v_mfma_f32_16x16x32_bf16 v[164:167], a[248:251], a[208:211], v[164:167]\n"
    "ds_read_b64_tr_b16 a[198:199], v27 offset:25088\n"
    "s_add_u32 s54, s54, 1\n"
    "s_and_b32 s54, s54, 15\n"
    "s_mul_i32 s56, s54, s29\n"
    "v_mfma_f32_16x16x32_bf16 v[200:203], a[208:211], a[252:255], v[200:203]\n"
    "ds_read_b64_tr_b16 a[200:201], v27 offset:25600\n"
    "v_mfma_f32_16x16x32_bf16 v[132:135], a[244:247], a[212:215], v[132:135]\n"
    "s_add_u32 m0, s60, 12288\n"
    "ds_read_b64_tr_b16 a[202:203], v27 offset:26112\n"
    "v_mfma_f32_16x16x32_bf16 v[168:171], a[248:251], a[212:215], v[168:171]\n"
    "ds_read_b64_tr_b16 a[204:205], v27 offset:26624\n"
    "v_mfma_f32_16x16x32_bf16 v[204:207], a[212:215], a[252:255], v[204:207]\n"
    "buffer_load_dwordx4 v21, s[8:11], s56 offen offset:0 lds\n"
    "ds_read_b64_tr_b16 a[206:207], v27 offset:27136\n"
    "v_mfma_f32_16x16x32_bf16 v[136:139], a[244:247], a[216:219], v[136:139]\n"
    "ds_read_b128 a[160:163], v25 offset:16384\n"
    "v_mfma_f32_16x16x32_bf16 v[172:175], a[248:251], a[216:219], v[172:175]\n"
    "buffer_load_dwordx4 v22, s[8:11], s56 offen offset:1024 lds\n"
    "ds_read_b128 a[164:167], v25 offset:0\n"
    "v_mfma_f32_16x16x32_bf16 v[208:211], a[216:219], a[252:255], v[208:211]\n"
    "ds_read_b128 a[168:171], v25 offset:2048\n"
    "buffer_load_dwordx4 v23, s[8:11], s56 offen offset:2048 lds\n"
    "v_mfma_f32_16x16x32_bf16 v[140:143], a[244:247], a[220:223], v[140:143]\n"
    "ds_read_b128 a[172:175], v25 offset:4096\n"
    "v_mfma_f32_16x16x32_bf16 v[176:179], a[248:251], a[220:223], v[176:179]\n"
    "ds_read_b128 a[176:179], v25 offset:6144\n"
    "s_add_u32 s55, s55, 1\n"
    "s_and_b32 s55, s55, 7\n"
    "s_lshl_b32 s57, s55, 7\n"
    "v_mfma_f32_16x16x32_bf16 v[212:215], a[220:223], a[252:255], v[212:215]\n"
    "ds_read_b128 a[180:183], v25 offset:8192\n"
    "v_mfma_f32_16x16x32_bf16 v[144:147], a[244:247], a[224:227], v[144:147]\n"
    "ds_read_b128 a[184:187], v25 offset:10240\n"
    "s_add_u32 m0, s59, 90112\n"
    "v_mfma_f32_16x16x32_bf16 v[180:183], a[248:251], a[224:227], v[180:183]\n"
    "ds_read_b128 a[188:191], v25 offset:12288\n"
    "v_mfma_f32_16x16x32_bf16 v[216:219], a[224:227], a[252:255], v[216:219]\n"
    "buffer_load_dwordx4 v17, s[4:7], s57 offen offset:0 lds\n"
    "ds_read_b128 a[192:195], v25 offset:14336\n"
    "v_mfma_f32_16x16x32_bf16 v[148:151], a[244:247], a[228:231], v[148:151]\n"
    "v_mfma_f32_16x16x32_bf16 v[184:187], a[248:251], a[228:231], v[184:187]\n"
    "v_mfma_f32_16x16x32_bf16 v[220:223], a[228:231], a[252:255], v[220:223]\n"
    "buffer_load_dwordx4 v18, s[4:7], s57 offen offset:1024 lds\n"
    "v_mfma_f32_16x16x32_bf16 v[152:155], a[244:247], a[232:235], v[152:155]\n"
    "v_mfma_f32_16x16x32_bf16 v[188:191], a[248:251], a[232:235], v[188:191]\n"
    "v_mfma_f32_16x16x32_bf16 v[224:227], a[232:235], a[252:255], v[224:227]\n"
    "v_mfma_f32_16x16x32_bf16 v[156:159], a[244:247], a[236:239], v[156:159]\n"
    "buffer_load_dwordx4 v19, s[4:7], s57 offen offset:2048 lds\n"
    "v_mfma_f32_16x16x32_bf16 v[192:195], a[248:251], a[236:239], v[192:195]\n"
    "v_mfma_f32_16x16x32_bf16 v[228:231], a[236:239], a[252:255], v[228:231]\n"
    "v_mfma_f32_16x16x32_bf16 v[160:163], a[244:247], a[240:243], v[160:163]\n"
    "v_mfma_f32_16x16x32_bf16 v[196:199], a[248:251], a[240:243], v[196:199]\n"
    "v_mfma_f32_16x16x32_bf16 v[232:235], a[240:243], a[252:255], v[232:235]\n"
    "s_waitcnt lgkmcnt(0)\n"
    "v_mfma_f32_16x16x32_bf16 v[128:131], a[196:199], a[160:163], v[128:131]\n"
    "s_waitcnt vmcnt(14)\n"
    "s_barrier\n"
    "ds_read_b64_tr_b16 a[244:245], v27 offset:36864\n"
    "v_mfma_f32_16x16x32_bf16 v[164:167], a[200:203], a[160:163], v[164:167]\n"
    "ds_read_b64_tr_b16 a[246:247], v27 offset:37376\n"
    "s_add_u32 m0, s59, 90112\n"
    "s_mul_i32 s58, s55, s28\n"
    "v_mfma_f32_16x16x32_bf16 v[200:203], a[160:163], a[204:207], v[200:203]\n"
    "ds_read_b64_tr_b16 a[248:249], v27 offset:37888\n"
    "v_mfma_f32_16x16x32_bf16 v[132:135], a[196:199], a[164:167], v[132:135]\n"
    "ds_read_b64_tr_b16 a[250:251], v27 offset:38400\n"
    "buffer_load_dwordx4 v20, s[4:7], s57 offen offset:3072 lds\n"
    "v_mfma_f32_16x16x32_bf16 v[168:171], a[200:203], a[164:167], v[168:171]\n"
    "ds_read_b64_tr_b16 a[252:253], v27 offset:38912\n"
    "v_mfma_f32_16x16x32_bf16 v[204:207], a[164:167], a[204:207], v[204:207]\n"
    "ds_read_b64_tr_b16 a[254:255], v27 offset:39424\n"
    "s_add_u32 m0, s61, 90112\n"
    "v_mfma_f32_16x16x32_bf16 v[136:139], a[196:199], a[168:171], v[136:139]\n"
    "ds_read_b128 a[208:211], v26 offset:16384\n"
    "v_mfma_f32_16x16x32_bf16 v[172:175], a[200:203], a[168:171], v[172:175]\n"
    "ds_read_b128 a[212:215], v26 offset:0\n"
    "buffer_load_dword v255, v28, s[20:23], 0 offen\n"
    "buffer_load_dwordx4 v24, s[36:39], s58 offen offset:0 lds\n"
    "v_mfma_f32_16x16x32_bf16 v[208:211], a[168:171], a[204:207], v[208:211]\n"
    "ds_read_b128 a[216:219], v26 offset:2048\n"
    "v_mfma_f32_16x16x32_bf16 v[140:143], a[196:199], a[172:175], v[140:143]\n"
    "ds_read_b128 a[220:223], v26 offset:4096\n"
    "s_add_u32 s54, s54, 1\n"
    "s_and_b32 s54, s54, 15\n"
    "s_mul_i32 s56, s54, s29\n"
    "v_mfma_f32_16x16x32_bf16 v[176:179], a[200:203], a[172:175], v[176:179]\n"
    "ds_read_b128 a[224:227], v26 offset:6144\n"
    "v_mfma_f32_16x16x32_bf16 v[212:215], a[172:175], a[204:207], v[212:215]\n"
    "s_mov_b64 s[20:21], s[32:33]\n"
    "s_mov_b32 s22, 1835008\n"
    "ds_read_b128 a[228:231], v26 offset:8192\n"
    "s_add_u32 m0, s60, 24576\n"
    "v_mfma_f32_16x16x32_bf16 v[144:147], a[196:199], a[176:179], v[144:147]\n"
    "ds_read_b128 a[232:235], v26 offset:10240\n"
    "v_mfma_f32_16x16x32_bf16 v[180:183], a[200:203], a[176:179], v[180:183]\n"
    "ds_read_b128 a[236:239], v26 offset:12288\n"
    "buffer_load_dwordx4 v21, s[8:11], s56 offen offset:0 lds\n"
    "v_mfma_f32_16x16x32_bf16 v[216:219], a[176:179], a[204:207], v[216:219]\n"
    "ds_read_b128 a[240:243], v26 offset:14336\n"
    "v_mfma_f32_16x16x32_bf16 v[148:151], a[196:199], a[180:183], v[148:151]\n"
    "v_mfma_f32_16x16x32_bf16 v[184:187], a[200:203], a[180:183], v[184:187]\n"
    "buffer_load_dwordx4 v22, s[8:11], s56 offen offset:1024 lds\n"
    "buffer_load_dword v255, v29, s[20:23], 0 offen\n"
    "v_mfma_f32_16x16x32_bf16 v[220:223], a[180:183], a[204:207], v[220:223]\n"
    "v_mfma_f32_16x16x32_bf16 v[152:155], a[196:199], a[184:187], v[152:155]\n"
    "v_mfma_f32_16x16x32_bf16 v[188:191], a[200:203], a[184:187], v[188:191]\n"
    "v_mfma_f32_16x16x32_bf16 v[224:227], a[184:187], a[204:207], v[224:227]\n"
    "buffer_load_dwordx4 v23, s[8:11], s56 offen offset:2048 lds\n"
    "v_mfma_f32_16x16x32_bf16 v[156:159], a[196:199], a[188:191], v[156:159]\n"
    "v_mfma_f32_16x16x32_bf16 v[192:195], a[200:203], a[188:191], v[192:195]\n"
    "v_mfma_f32_16x16x32_bf16 v[228:231], a[188:191], a[204:207], v[228:231]\n"
    "v_mfma_f32_16x16x32_bf16 v[160:163], a[196:199], a[192:195], v[160:163]\n"
    "v_mfma_f32_16x16x32_bf16 v[196:199], a[200:203], a[192:195], v[196:199]\n"
    "v_mfma_f32_16x16x32_bf16 v[232:235], a[192:195], a[204:207], v[232:235]\n"
    "s_waitcnt lgkmcnt(0)\n"
    "v_mfma_f32_16x16x32_bf16 v[128:131], a[244:247], a[208:211], v[128:131]\n"
    "s_waitcnt vmcnt(13)\n"
    "s_barrier\n"
    "ds_read_b64_tr_b16 a[196:197], v27 offset:0\n"
    "v_mfma_f32_16x16x32_bf16 v[164:167], a[248:251], a[208:211], v[164:167]\n"
    "ds_read_b64_tr_b16 a[198:199], v27 offset:512\n"
    "s_add_u32 s54, s54, 1\n"
    "s_and_b32 s54, s54, 15\n"
    "s_mul_i32 s56, s54, s29\n"
    "v_mfma_f32_16x16x32_bf16 v[200:203], a[208:211], a[252:255], v[200:203]\n"
    "ds_read_b64_tr_b16 a[200:201], v27 offset:1024\n"
    "v_mfma_f32_16x16x32_bf16 v[132:135], a[244:247], a[212:215], v[132:135]\n"
    "s_add_u32 m0, s60, 36864\n"
    "ds_read_b64_tr_b16 a[202:203], v27 offset:1536\n"
    "v_mfma_f32_16x16x32_bf16 v[168:171], a[248:251], a[212:215], v[168:171]\n"
    "ds_read_b64_tr_b16 a[204:205], v27 offset:2048\n"
    "v_mfma_f32_16x16x32_bf16 v[204:207], a[212:215], a[252:255], v[204:207]\n"
    "buffer_load_dwordx4 v21, s[8:11], s56 offen offset:0 lds\n"
    "ds_read_b64_tr_b16 a[206:207], v27 offset:2560\n"
    "v_mfma_f32_16x16x32_bf16 v[136:139], a[244:247], a[216:219], v[136:139]\n"
    "ds_read_b128 a[160:163], v25 offset:36864\n"
    "v_mfma_f32_16x16x32_bf16 v[172:175], a[248:251], a[216:219], v[172:175]\n"
    "buffer_load_dwordx4 v22, s[8:11], s56 offen offset:1024 lds\n"
    "ds_read_b128 a[164:167], v25 offset:20480\n"
    "v_mfma_f32_16x16x32_bf16 v[208:211], a[216:219], a[252:255], v[208:211]\n"
    "ds_read_b128 a[168:171], v25 offset:22528\n"
    "buffer_load_dwordx4 v23, s[8:11], s56 offen offset:2048 lds\n"
    "v_mfma_f32_16x16x32_bf16 v[140:143], a[244:247], a[220:223], v[140:143]\n"
    "ds_read_b128 a[172:175], v25 offset:24576\n"
    "v_mfma_f32_16x16x32_bf16 v[176:179], a[248:251], a[220:223], v[176:179]\n"
    "ds_read_b128 a[176:179], v25 offset:26624\n"
    "s_add_u32 s55, s55, 1\n"
    "s_and_b32 s55, s55, 7\n"
    "s_lshl_b32 s57, s55, 7\n"
    "v_mfma_f32_16x16x32_bf16 v[212:215], a[220:223], a[252:255], v[212:215]\n"
    "ds_read_b128 a[180:183], v25 offset:28672\n"
    "v_mfma_f32_16x16x32_bf16 v[144:147], a[244:247], a[224:227], v[144:147]\n"
    "ds_read_b128 a[184:187], v25 offset:30720\n"
    "s_add_u32 m0, s59, 49152\n"
    "v_mfma_f32_16x16x32_bf16 v[180:183], a[248:251], a[224:227], v[180:183]\n"
    "ds_read_b128 a[188:191], v25 offset:32768\n"
    "v_mfma_f32_16x16x32_bf16 v[216:219], a[224:227], a[252:255], v[216:219]\n"
    "buffer_load_dwordx4 v17, s[4:7], s57 offen offset:0 lds\n"
    "ds_read_b128 a[192:195], v25 offset:34816\n"
    "v_mfma_f32_16x16x32_bf16 v[148:151], a[244:247], a[228:231], v[148:151]\n"
    "v_mfma_f32_16x16x32_bf16 v[184:187], a[248:251], a[228:231], v[184:187]\n"
    "v_mfma_f32_16x16x32_bf16 v[220:223], a[228:231], a[252:255], v[220:223]\n"
    "buffer_load_dwordx4 v18, s[4:7], s57 offen offset:1024 lds\n"
    "v_mfma_f32_16x16x32_bf16 v[152:155], a[244:247], a[232:235], v[152:155]\n"
    "v_mfma_f32_16x16x32_bf16 v[188:191], a[248:251], a[232:235], v[188:191]\n"
    "v_mfma_f32_16x16x32_bf16 v[224:227], a[232:235], a[252:255], v[224:227]\n"
    "v_mfma_f32_16x16x32_bf16 v[156:159], a[244:247], a[236:239], v[156:159]\n"
    "buffer_load_dwordx4 v19, s[4:7], s57 offen offset:2048 lds\n"
    "v_mfma_f32_16x16x32_bf16 v[192:195], a[248:251], a[236:239], v[192:195]\n"
    "v_mfma_f32_16x16x32_bf16 v[228:231], a[236:239], a[252:255], v[228:231]\n"
    "v_mfma_f32_16x16x32_bf16 v[160:163], a[244:247], a[240:243], v[160:163]\n"
    "v_mfma_f32_16x16x32_bf16 v[196:199], a[248:251], a[240:243], v[196:199]\n"
    "v_mfma_f32_16x16x32_bf16 v[232:235], a[240:243], a[252:255], v[232:235]\n"
    "s_waitcnt lgkmcnt(0)\n"
    "v_mfma_f32_16x16x32_bf16 v[128:131], a[196:199], a[160:163], v[128:131]\n"
    "s_waitcnt vmcnt(16)\n"
    "s_barrier\n"
    "ds_read_b64_tr_b16 a[244:245], v27 offset:12288\n"
    "v_mfma_f32_16x16x32_bf16 v[164:167], a[200:203], a[160:163], v[164:167]\n"
    "ds_read_b64_tr_b16 a[246:247], v27 offset:12800\n"
    "s_add_u32 m0, s59, 49152\n"
    "s_mul_i32 s58, s55, s28\n"
    "v_mfma_f32_16x16x32_bf16 v[200:203], a[160:163], a[204:207], v[200:203]\n"
    "ds_read_b64_tr_b16 a[248:249], v27 offset:13312\n"
    "v_mfma_f32_16x16x32_bf16 v[132:135], a[196:199], a[164:167], v[132:135]\n"
    "ds_read_b64_tr_b16 a[250:251], v27 offset:13824\n"
    "buffer_load_dwordx4 v20, s[4:7], s57 offen offset:3072 lds\n"
    "v_mfma_f32_16x16x32_bf16 v[168:171], a[200:203], a[164:167], v[168:171]\n"
    "ds_read_b64_tr_b16 a[252:253], v27 offset:14336\n"
    "v_mfma_f32_16x16x32_bf16 v[204:207], a[164:167], a[204:207], v[204:207]\n"
    "ds_read_b64_tr_b16 a[254:255], v27 offset:14848\n"
    "s_add_u32 m0, s61, 49152\n"
    "v_mfma_f32_16x16x32_bf16 v[136:139], a[196:199], a[168:171], v[136:139]\n"
    "ds_read_b128 a[208:211], v26 offset:36864\n"
    "v_mfma_f32_16x16x32_bf16 v[172:175], a[200:203], a[168:171], v[172:175]\n"
    "ds_read_b128 a[212:215], v26 offset:20480\n"
    "buffer_load_dwordx4 v24, s[36:39], s58 offen offset:0 lds\n"
    "v_mfma_f32_16x16x32_bf16 v[208:211], a[168:171], a[204:207], v[208:211]\n"
    "ds_read_b128 a[216:219], v26 offset:22528\n"
    "v_mfma_f32_16x16x32_bf16 v[140:143], a[196:199], a[172:175], v[140:143]\n"
    "ds_read_b128 a[220:223], v26 offset:24576\n"
    "s_add_u32 s54, s54, 1\n"
    "s_and_b32 s54, s54, 15\n"
    "s_mul_i32 s56, s54, s29\n"
    "v_mfma_f32_16x16x32_bf16 v[176:179], a[200:203], a[172:175], v[176:179]\n"
    "ds_read_b128 a[224:227], v26 offset:26624\n"
    "v_mfma_f32_16x16x32_bf16 v[212:215], a[172:175], a[204:207], v[212:215]\n"
    "ds_read_b128 a[228:231], v26 offset:28672\n"
    "s_add_u32 m0, s60, 0\n"
    "v_mfma_f32_16x16x32_bf16 v[144:147], a[196:199], a[176:179], v[144:147]\n"
    "ds_read_b128 a[232:235], v26 offset:30720\n"
    "v_mfma_f32_16x16x32_bf16 v[180:183], a[200:203], a[176:179], v[180:183]\n"
    "ds_read_b128 a[236:239], v26 offset:32768\n"
    "buffer_load_dwordx4 v21, s[8:11], s56 offen offset:0 lds\n"
    "v_mfma_f32_16x16x32_bf16 v[216:219], a[176:179], a[204:207], v[216:219]\n"
    "ds_read_b128 a[240:243], v26 offset:34816\n"
    "v_mfma_f32_16x16x32_bf16 v[148:151], a[196:199], a[180:183], v[148:151]\n"
    "v_mfma_f32_16x16x32_bf16 v[184:187], a[200:203], a[180:183], v[184:187]\n"
    "buffer_load_dwordx4 v22, s[8:11], s56 offen offset:1024 lds\n"
    "v_mfma_f32_16x16x32_bf16 v[220:223], a[180:183], a[204:207], v[220:223]\n"
    "v_mfma_f32_16x16x32_bf16 v[152:155], a[196:199], a[184:187], v[152:155]\n"
    "v_mfma_f32_16x16x32_bf16 v[188:191], a[200:203], a[184:187], v[188:191]\n"
    "v_mfma_f32_16x16x32_bf16 v[224:227], a[184:187], a[204:207], v[224:227]\n"
    "buffer_load_dwordx4 v23, s[8:11], s56 offen offset:2048 lds\n"
    "v_mfma_f32_16x16x32_bf16 v[156:159], a[196:199], a[188:191], v[156:159]\n"
    "v_mfma_f32_16x16x32_bf16 v[192:195], a[200:203], a[188:191], v[192:195]\n"
    "v_mfma_f32_16x16x32_bf16 v[228:231], a[188:191], a[204:207], v[228:231]\n"
    "v_mfma_f32_16x16x32_bf16 v[160:163], a[196:199], a[192:195], v[160:163]\n"
    "v_mfma_f32_16x16x32_bf16 v[196:199], a[200:203], a[192:195], v[196:199]\n"
    "v_mfma_f32_16x16x32_bf16 v[232:235], a[192:195], a[204:207], v[232:235]\n"
    "s_waitcnt lgkmcnt(0)\n"
    "v_mfma_f32_16x16x32_bf16 v[128:131], a[244:247], a[208:211], v[128:131]\n"
    "s_waitcnt vmcnt(11)\n"
    "s_barrier\n"
    "ds_read_b64_tr_b16 a[196:197], v27 offset:24576\n"
    "v_mfma_f32_16x16x32_bf16 v[164:167], a[248:251], a[208:211], v[164:167]\n"
    "ds_read_b64_tr_b16 a[198:199], v27 offset:25088\n"
    "s_add_u32 s54, s54, 1\n"
    "s_and_b32 s54, s54, 15\n"
    "s_mul_i32 s56, s54, s29\n"
    "v_mfma_f32_16x16x32_bf16 v[200:203], a[208:211], a[252:255], v[200:203]\n"
    "ds_read_b64_tr_b16 a[200:201], v27 offset:25600\n"
    "v_mfma_f32_16x16x32_bf16 v[132:135], a[244:247], a[212:215], v[132:135]\n"
    "s_add_u32 m0, s60, 12288\n"
    "ds_read_b64_tr_b16 a[202:203], v27 offset:26112\n"
    "v_mfma_f32_16x16x32_bf16 v[168:171], a[248:251], a[212:215], v[168:171]\n"
    "ds_read_b64_tr_b16 a[204:205], v27 offset:26624\n"
    "v_mfma_f32_16x16x32_bf16 v[204:207], a[212:215], a[252:255], v[204:207]\n"
    "buffer_load_dwordx4 v21, s[8:11], s56 offen offset:0 lds\n"
    "ds_read_b64_tr_b16 a[206:207], v27 offset:27136\n"
    "v_mfma_f32_16x16x32_bf16 v[136:139], a[244:247], a[216:219], v[136:139]\n"
    "ds_read_b128 a[160:163], v25 offset:57344\n"
    "v_mfma_f32_16x16x32_bf16 v[172:175], a[248:251], a[216:219], v[172:175]\n"
    "buffer_load_dwordx4 v22, s[8:11], s56 offen offset:1024 lds\n"
    "buffer_load_dwordx2 v[108:109], v34, s[16:19], 0 offen sc1\n"
    "ds_read_b128 a[164:167], v25 offset:40960\n"
    "v_mfma_f32_16x16x32_bf16 v[208:211], a[216:219], a[252:255], v[208:211]\n"
    "ds_read_b128 a[168:171], v25 offset:43008\n"
    "buffer_load_dwordx4 v23, s[8:11], s56 offen offset:2048 lds\n"
    "v_mfma_f32_16x16x32_bf16 v[140:143], a[244:247], a[220:223], v[140:143]\n"
    "ds_read_b128 a[172:175], v25 offset:45056\n"
    "v_mfma_f32_16x16x32_bf16 v[176:179], a[248:251], a[220:223], v[176:179]\n"
    "ds_read_b128 a[176:179], v25 offset:47104\n"
    "s_add_u32 s55, s55, 1\n"
    "s_and_b32 s55, s55, 7\n"
    "s_lshl_b32 s57, s55, 7\n"
    "v_mfma_f32_16x16x32_bf16 v[212:215], a[220:223], a[252:255], v[212:215]\n"
    "ds_read_b128 a[180:183], v25 offset:49152\n"
    "v_mfma_f32_16x16x32_bf16 v[144:147], a[244:247], a[224:227], v[144:147]\n"
    "ds_read_b128 a[184:187], v25 offset:51200\n"
    "s_add_u32 m0, s59, 69632\n"
    "v_mfma_f32_16x16x32_bf16 v[180:183], a[248:251], a[224:227], v[180:183]\n"
    "ds_read_b128 a[188:191], v25 offset:53248\n"
    "v_mfma_f32_16x16x32_bf16 v[216:219], a[224:227], a[252:255], v[216:219]\n"
    "buffer_load_dwordx4 v17, s[4:7], s57 offen offset:0 lds\n"
    "ds_read_b128 a[192:195], v25 offset:55296\n"
    "v_mfma_f32_16x16x32_bf16 v[148:151], a[244:247], a[228:231], v[148:151]\n"
    "v_mfma_f32_16x16x32_bf16 v[184:187], a[248:251], a[228:231], v[184:187]\n"
    "v_mfma_f32_16x16x32_bf16 v[220:223], a[228:231], a[252:255], v[220:223]\n"
    "buffer_load_dwordx4 v18, s[4:7], s57 offen offset:1024 lds\n"
    "v_mfma_f32_16x16x32_bf16 v[152:155], a[244:247], a[232:235], v[152:155]\n"
    "v_mfma_f32_16x16x32_bf16 v[188:191], a[248:251], a[232:235], v[188:191]\n"
    "v_mfma_f32_16x16x32_bf16 v[224:227], a[232:235], a[252:255], v[224:227]\n"
    "v_mfma_f32_16x16x32_bf16 v[156:159], a[244:247], a[236:239], v[156:159]\n"
    "buffer_load_dwordx4 v19, s[4:7], s57 offen offset:2048 lds\n"
    "v_mfma_f32_16x16x32_bf16 v[192:195], a[248:251], a[236:239], v[192:195]\n"
    "v_mfma_f32_16x16x32_bf16 v[228:231], a[236:239], a[252:255], v[228:231]\n"
    "v_mfma_f32_16x16x32_bf16 v[160:163], a[244:247], a[240:243], v[160:163]\n"
    "v_mfma_f32_16x16x32_bf16 v[196:199], a[248:251], a[240:243], v[196:199]\n"
    "v_mfma_f32_16x16x32_bf16 v[232:235], a[240:243], a[252:255], v[232:235]\n"
    "s_waitcnt lgkmcnt(0)\n"
    "v_mfma_f32_16x16x32_bf16 v[128:131], a[196:199], a[160:163], v[128:131]\n"
    "s_waitcnt vmcnt(15)\n"
    "s_barrier\n"
    "ds_read_b64_tr_b16 a[244:245], v27 offset:36864\n"
    "v_mfma_f32_16x16x32_bf16 v[164:167], a[200:203], a[160:163], v[164:167]\n"
    "ds_read_b64_tr_b16 a[246:247], v27 offset:37376\n"
    "s_add_u32 m0, s59, 69632\n"
    "s_mul_i32 s58, s55, s28\n"
    "v_mfma_f32_16x16x32_bf16 v[200:203], a[160:163], a[204:207], v[200:203]\n"
    "ds_read_b64_tr_b16 a[248:249], v27 offset:37888\n"
    "v_mfma_f32_16x16x32_bf16 v[132:135], a[196:199], a[164:167], v[132:135]\n"
    "ds_read_b64_tr_b16 a[250:251], v27 offset:38400\n"
    "buffer_load_dwordx4 v20, s[4:7], s57 offen offset:3072 lds\n"
    "v_mfma_f32_16x16x32_bf16 v[168:171], a[200:203], a[164:167], v[168:171]\n"
    "ds_read_b64_tr_b16 a[252:253], v27 offset:38912\n"
    "v_mfma_f32_16x16x32_bf16 v[204:207], a[164:167], a[204:207], v[204:207]\n"
    "ds_read_b64_tr_b16 a[254:255], v27 offset:39424\n"
    "s_add_u32 m0, s61, 69632\n"
    "v_mfma_f32_16x16x32_bf16 v[136:139], a[196:199], a[168:171], v[136:139]\n"
    "ds_read_b128 a[208:211], v26 offset:57344\n"
    "v_mfma_f32_16x16x32_bf16 v[172:175], a[200:203], a[168:171], v[172:175]\n"
    "ds_read_b128 a[212:215], v26 offset:40960\n"
    "buffer_load_dwordx4 v24, s[36:39], s58 offen offset:0 lds\n"
    "v_mfma_f32_16x16x32_bf16 v[208:211], a[168:171], a[204:207], v[208:211]\n"
    "ds_read_b128 a[216:219], v26 offset:43008\n"
    "v_mfma_f32_16x16x32_bf16 v[140:143], a[196:199], a[172:175], v[140:143]\n"
    "ds_read_b128 a[220:223], v26 offset:45056\n"
    "s_add_u32 s54, s54, 1\n"
    "s_and_b32 s54, s54, 15\n"
    "s_mul_i32 s56, s54, s29\n"
    "v_mfma_f32_16x16x32_bf16 v[176:179], a[200:203], a[172:175], v[176:179]\n"
    "ds_read_b128 a[224:227], v26 offset:47104\n"
    "v_mfma_f32_16x16x32_bf16 v[212:215], a[172:175], a[204:207], v[212:215]\n"
    "ds_read_b128 a[228:231], v26 offset:49152\n"
    "s_add_u32 m0, s60, 24576\n"
    "v_mfma_f32_16x16x32_bf16 v[144:147], a[196:199], a[176:179], v[144:147]\n"
    "ds_read_b128 a[232:235], v26 offset:51200\n"
    "v_mfma_f32_16x16x32_bf16 v[180:183], a[200:203], a[176:179], v[180:183]\n"
    "ds_read_b128 a[236:239], v26 offset:53248\n"
    "buffer_load_dwordx4 v21, s[8:11], s56 offen offset:0 lds\n"
    "v_mfma_f32_16x16x32_bf16 v[216:219], a[176:179], a[204:207], v[216:219]\n"
    "ds_read_b128 a[240:243], v26 offset:55296\n"
    "v_mfma_f32_16x16x32_bf16 v[148:151], a[196:199], a[180:183], v[148:151]\n"
    "v_mfma_f32_16x16x32_bf16 v[184:187], a[200:203], a[180:183], v[184:187]\n"
    "buffer_load_dwordx4 v22, s[8:11], s56 offen offset:1024 lds\n"
    "v_mfma_f32_16x16x32_bf16 v[220:223], a[180:183], a[204:207], v[220:223]\n"
    "v_mfma_f32_16x16x32_bf16 v[152:155], a[196:199], a[184:187], v[152:155]\n"
    "v_mfma_f32_16x16x32_bf16 v[188:191], a[200:203], a[184:187], v[188:191]\n"
    "v_mfma_f32_16x16x32_bf16 v[224:227], a[184:187], a[204:207], v[224:227]\n"
    "buffer_load_dwordx4 v23, s[8:11], s56 offen offset:2048 lds\n"
    "v_mfma_f32_16x16x32_bf16 v[156:159], a[196:199], a[188:191], v[156:159]\n"
    "v_mfma_f32_16x16x32_bf16 v[192:195], a[200:203], a[188:191], v[192:195]\n"
    "v_mfma_f32_16x16x32_bf16 v[228:231], a[188:191], a[204:207], v[228:231]\n"
    "v_mfma_f32_16x16x32_bf16 v[160:163], a[196:199], a[192:195], v[160:163]\n"
    "v_mfma_f32_16x16x32_bf16 v[196:199], a[200:203], a[192:195], v[196:199]\n"
    "v_mfma_f32_16x16x32_bf16 v[232:235], a[192:195], a[204:207], v[232:235]\n"
    "s_waitcnt lgkmcnt(0)\n"
    "v_mfma_f32_16x16x32_bf16 v[128:131], a[244:247], a[208:211], v[128:131]\n"
    "s_waitcnt vmcnt(12)\n"
    "s_barrier\n"
    "ds_read_b64_tr_b16 a[196:197], v27 offset:0\n"
    "v_mfma_f32_16x16x32_bf16 v[164:167], a[248:251], a[208:211], v[164:167]\n"
    "ds_read_b64_tr_b16 a[198:199], v27 offset:512\n"
    "s_add_u32 s54, s54, 1\n"
    "s_and_b32 s54, s54, 15\n"
    "s_mul_i32 s56, s54, s29\n"
    "v_mfma_f32_16x16x32_bf16 v[200:203], a[208:211], a[252:255], v[200:203]\n"
    "ds_read_b64_tr_b16 a[200:201], v27 offset:1024\n"
    "v_mfma_f32_16x16x32_bf16 v[132:135], a[244:247], a[212:215], v[132:135]\n"
    "ds_read_b64_tr_b16 a[202:203], v27 offset:1536\n"
    "v_mfma_f32_16x16x32_bf16 v[168:171], a[248:251], a[212:215], v[168:171]\n"
    "ds_read_b64_tr_b16 a[204:205], v27 offset:2048\n"
    "v_mfma_f32_16x16x32_bf16 v[204:207], a[212:215], a[252:255], v[204:207]\n"
    "s_add_u32 m0, s60, 36864\n"
    "ds_read_b64_tr_b16 a[206:207], v27 offset:2560\n"
    "v_mfma_f32_16x16x32_bf16 v[136:139], a[244:247], a[216:219], v[136:139]\n"
    "ds_read_b128 a[160:163], v25 offset:16384\n"
    "v_mfma_f32_16x16x32_bf16 v[172:175], a[248:251], a[216:219], v[172:175]\n"
    "ds_read_b128 a[164:167], v25 offset:0\n"
    "v_mfma_f32_16x16x32_bf16 v[208:211], a[216:219], a[252:255], v[208:211]\n"
    "ds_read_b128 a[168:171], v25 offset:2048\n"
    "buffer_load_dwordx4 v21, s[8:11], s56 offen offset:0 lds\n"
    "v_mfma_f32_16x16x32_bf16 v[140:143], a[244:247], a[220:223], v[140:143]\n"
    "ds_read_b128 a[172:175], v25 offset:4096\n"
    "v_mfma_f32_16x16x32_bf16 v[176:179], a[248:251], a[220:223], v[176:179]\n"
    "ds_read_b128 a[176:179], v25 offset:6144\n"
    "v_mfma_f32_16x16x32_bf16 v[212:215], a[220:223], a[252:255], v[212:215]\n"
    "ds_read_b128 a[180:183], v25 offset:8192\n"
    "v_mfma_f32_16x16x32_bf16 v[144:147], a[244:247], a[224:227], v[144:147]\n"
    "ds_read_b128 a[184:187], v25 offset:10240\n"
    "buffer_load_dwordx4 v22, s[8:11], s56 offen offset:1024 lds\n"
    "v_mfma_f32_16x16x32_bf16 v[180:183], a[248:251], a[224:227], v[180:183]\n"
    "ds_read_b128 a[188:191], v25 offset:12288\n"
    "v_mfma_f32_16x16x32_bf16 v[216:219], a[224:227], a[252:255], v[216:219]\n"
    "ds_read_b128 a[192:195], v25 offset:14336\n"
    "v_mfma_f32_16x16x32_bf16 v[148:151], a[244:247], a[228:231], v[148:151]\n"
    "v_mfma_f32_16x16x32_bf16 v[184:187], a[248:251], a[228:231], v[184:187]\n"
    "v_mfma_f32_16x16x32_bf16 v[220:223], a[228:231], a[252:255], v[220:223]\n"
    "buffer_load_dwordx4 v23, s[8:11], s56 offen offset:2048 lds\n"
    "v_mfma_f32_16x16x32_bf16 v[152:155], a[244:247], a[232:235], v[152:155]\n"
    "v_mfma_f32_16x16x32_bf16 v[188:191], a[248:251], a[232:235], v[188:191]\n"
    "v_mfma_f32_16x16x32_bf16 v[224:227], a[232:235], a[252:255], v[224:227]\n"
    "v_mfma_f32_16x16x32_bf16 v[156:159], a[244:247], a[236:239], v[156:159]\n"
    "v_mfma_f32_16x16x32_bf16 v[192:195], a[248:251], a[236:239], v[192:195]\n"
    "v_mfma_f32_16x16x32_bf16 v[228:231], a[236:239], a[252:255], v[228:231]\n"
    "v_mfma_f32_16x16x32_bf16 v[160:163], a[244:247], a[240:243], v[160:163]\n"
    "v_mfma_f32_16x16x32_bf16 v[196:199], a[248:251], a[240:243], v[196:199]\n"
    "v_mfma_f32_16x16x32_bf16 v[232:235], a[240:243], a[252:255], v[232:235]\n"
    "s_waitcnt lgkmcnt(0)\n"
    "v_mfma_f32_16x16x32_bf16 v[128:131], a[196:199], a[160:163], v[128:131]\n"
    "s_waitcnt vmcnt(11)\n"
    "s_barrier\n"
    "ds_read_b64_tr_b16 a[244:245], v27 offset:12288\n"
    "v_mfma_f32_16x16x32_bf16 v[164:167], a[200:203], a[160:163], v[164:167]\n"
    "ds_read_b64_tr_b16 a[246:247], v27 offset:12800\n"
    "s_mov_b32 s50, 0\n"
    "s_add_u32 m0, s59, 114688\n"
    "v_mfma_f32_16x16x32_bf16 v[200:203], a[160:163], a[204:207], v[200:203]\n"
    "ds_read_b64_tr_b16 a[248:249], v27 offset:13312\n"
    "v_mfma_f32_16x16x32_bf16 v[132:135], a[196:199], a[164:167], v[132:135]\n"
    "ds_read_b64_tr_b16 a[250:251], v27 offset:13824\n"
    "v_mfma_f32_16x16x32_bf16 v[168:171], a[200:203], a[164:167], v[168:171]\n"
    "ds_read_b64_tr_b16 a[252:253], v27 offset:14336\n"
    "v_mfma_f32_16x16x32_bf16 v[204:207], a[164:167], a[204:207], v[204:207]\n"
    "buffer_load_dwordx4 v2, s[12:15], s50 offen offset:0 lds\n"
    "ds_read_b64_tr_b16 a[254:255], v27 offset:14848\n"
    "v_mfma_f32_16x16x32_bf16 v[136:139], a[196:199], a[168:171], v[136:139]\n"
    "ds_read_b128 a[208:211], v26 offset:16384\n"
    "v_mfma_f32_16x16x32_bf16 v[172:175], a[200:203], a[168:171], v[172:175]\n"
    "ds_read_b128 a[212:215], v26 offset:0\n"
    "v_mfma_f32_16x16x32_bf16 v[208:211], a[168:171], a[204:207], v[208:211]\n"
    "ds_read_b128 a[216:219], v26 offset:2048\n"
    "buffer_load_dwordx4 v3, s[12:15], s50 offen offset:1024 lds\n"
    "v_mfma_f32_16x16x32_bf16 v[140:143], a[196:199], a[172:175], v[140:143]\n"
    "ds_read_b128 a[220:223], v26 offset:4096\n"
    "v_mfma_f32_16x16x32_bf16 v[176:179], a[200:203], a[172:175], v[176:179]\n"
    "ds_read_b128 a[224:227], v26 offset:6144\n"
    "v_mfma_f32_16x16x32_bf16 v[212:215], a[172:175], a[204:207], v[212:215]\n"
    "ds_read_b128 a[228:231], v26 offset:8192\n"
    "v_mfma_f32_16x16x32_bf16 v[144:147], a[196:199], a[176:179], v[144:147]\n"
    "ds_read_b128 a[232:235], v26 offset:10240\n"
    "buffer_load_dwordx4 v4, s[12:15], s50 offen offset:2048 lds\n"
    "v_mfma_f32_16x16x32_bf16 v[180:183], a[200:203], a[176:179], v[180:183]\n"
    "ds_read_b128 a[236:239], v26 offset:12288\n"
    "v_mfma_f32_16x16x32_bf16 v[216:219], a[176:179], a[204:207], v[216:219]\n"
    "ds_read_b128 a[240:243], v26 offset:14336\n"
    "v_mfma_f32_16x16x32_bf16 v[148:151], a[196:199], a[180:183], v[148:151]\n"
    "v_mfma_f32_16x16x32_bf16 v[184:187], a[200:203], a[180:183], v[184:187]\n"
    "v_mfma_f32_16x16x32_bf16 v[220:223], a[180:183], a[204:207], v[220:223]\n"
    "buffer_load_dwordx4 v5, s[12:15], s50 offen offset:3072 lds\n"
    "v_mfma_f32_16x16x32_bf16 v[152:155], a[196:199], a[184:187], v[152:155]\n"
    "v_mfma_f32_16x16x32_bf16 v[188:191], a[200:203], a[184:187], v[188:191]\n"
    "v_mfma_f32_16x16x32_bf16 v[224:227], a[184:187], a[204:207], v[224:227]\n"
    "v_mfma_f32_16x16x32_bf16 v[156:159], a[196:199], a[188:191], v[156:159]\n"
    "v_mfma_f32_16x16x32_bf16 v[192:195], a[200:203], a[188:191], v[192:195]\n"
    "v_mfma_f32_16x16x32_bf16 v[228:231], a[188:191], a[204:207], v[228:231]\n"
    "v_mfma_f32_16x16x32_bf16 v[160:163], a[196:199], a[192:195], v[160:163]\n"
    "v_mfma_f32_16x16x32_bf16 v[196:199], a[200:203], a[192:195], v[196:199]\n"
    "v_mfma_f32_16x16x32_bf16 v[232:235], a[192:195], a[204:207], v[232:235]\n"
    "s_waitcnt lgkmcnt(0)\n"
    "v_mfma_f32_16x16x32_bf16 v[128:131], a[244:247], a[208:211], v[128:131]\n"
    "s_waitcnt vmcnt(7)\n"
    "s_barrier\n"
    "ds_read_b64_tr_b16 a[196:197], v27 offset:24576\n"
    "v_mfma_f32_16x16x32_bf16 v[164:167], a[248:251], a[208:211], v[164:167]\n"
    "ds_read_b64_tr_b16 a[198:199], v27 offset:25088\n"
    "s_mov_b32 s50, 32768\n"
    "s_add_u32 m0, s59, 131072\n"
    "v_mfma_f32_16x16x32_bf16 v[200:203], a[208:211], a[252:255], v[200:203]\n"
    "ds_read_b64_tr_b16 a[200:201], v27 offset:25600\n"
    "v_mfma_f32_16x16x32_bf16 v[132:135], a[244:247], a[212:215], v[132:135]\n"
    "ds_read_b64_tr_b16 a[202:203], v27 offset:26112\n"
    "v_mfma_f32_16x16x32_bf16 v[168:171], a[248:251], a[212:215], v[168:171]\n"
    "ds_read_b64_tr_b16 a[204:205], v27 offset:26624\n"
    "v_mfma_f32_16x16x32_bf16 v[204:207], a[212:215], a[252:255], v[204:207]\n"
    "buffer_load_dwordx4 v2, s[12:15], s50 offen offset:0 lds\n"
    "ds_read_b64_tr_b16 a[206:207], v27 offset:27136\n"
    "v_mfma_f32_16x16x32_bf16 v[136:139], a[244:247], a[216:219], v[136:139]\n"
    "ds_read_b128 a[160:163], v25 offset:36864\n"
    "v_mfma_f32_16x16x32_bf16 v[172:175], a[248:251], a[216:219], v[172:175]\n"
    "s_waitcnt vmcnt(17)\n"
    "v_cmp_ne_u32 vcc, s66, v109\n"
    "s_nop 0\n"
    "s_cbranch_vccnz L_gre_attn\n"
    "L_gok_attn:\n"
    "ds_read_b128 a[164:167], v25 offset:20480\n"
    "v_mfma_f32_16x16x32_bf16 v[208:211], a[216:219], a[252:255], v[208:211]\n"
    "v_lshlrev_b32 v106, 16, v108\n"
    "ds_read_b128 a[168:171], v25 offset:22528\n"
    "buffer_load_dwordx4 v3, s[12:15], s50 offen offset:1024 lds\n"
    "v_mfma_f32_16x16x32_bf16 v[140:143], a[244:247], a[220:223], v[140:143]\n"
    "v_and_b32 v107, 0xffff0000, v108\n"
    "ds_read_b128 a[172:175], v25 offset:24576\n"
    "v_mfma_f32_16x16x32_bf16 v[176:179], a[248:251], a[220:223], v[176:179]\n"
    "ds_read_b128 a[176:179], v25 offset:26624\n"
    "v_xor_b32 v106, 0x80000000, v106\n"
    "v_mfma_f32_16x16x32_bf16 v[212:215], a[220:223], a[252:255], v[212:215]\n"
    "ds_read_b128 a[180:183], v25 offset:28672\n"
    "v_mfma_f32_16x16x32_bf16 v[144:147], a[244:247], a[224:227], v[144:147]\n"
    "v_rcp_f32 v107, v107\n"
    "ds_read_b128 a[184:187], v25 offset:30720\n"
    "buffer_load_dwordx4 v4, s[12:15], s50 offen offset:2048 lds\n"
    "v_mfma_f32_16x16x32_bf16 v[180:183], a[248:251], a[224:227], v[180:183]\n"
    "v_lshlrev_b32 v105, 1, v35\n"
    "ds_read_b128 a[188:191], v25 offset:32768\n"
    "v_mfma_f32_16x16x32_bf16 v[216:219], a[224:227], a[252:255], v[216:219]\n"
    "v_add_u32 v105, 110592, v105\n"
    "ds_read_b128 a[192:195], v25 offset:34816\n"
    "v_mfma_f32_16x16x32_bf16 v[148:151], a[244:247], a[228:231], v[148:151]\n"
    "v_mfma_f32_16x16x32_bf16 v[184:187], a[248:251], a[228:231], v[184:187]\n"
    "ds_write_b64 v105, v[106:107]\n"
    "v_mfma_f32_16x16x32_bf16 v[220:223], a[228:231], a[252:255], v[220:223]\n"
    "buffer_load_dwordx4 v5, s[12:15], s50 offen offset:3072 lds\n"
    "v_mfma_f32_16x16x32_bf16 v[152:155], a[244:247], a[232:235], v[152:155]\n"
    "v_add_u32 v105, 110592, v35\n"
    "v_mfma_f32_16x16x32_bf16 v[188:191], a[248:251], a[232:235], v[188:191]\n"
    "v_mfma_f32_16x16x32_bf16 v[224:227], a[232:235], a[252:255], v[224:227]\n"
    "v_mfma_f32_16x16x32_bf16 v[156:159], a[244:247], a[236:239], v[156:159]\n"
    "ds_write_b32 v105, v106 offset:1152\n"
    "v_mfma_f32_16x16x32_bf16 v[192:195], a[248:251], a[236:239], v[192:195]\n"
    "v_mfma_f32_16x16x32_bf16 v[228:231], a[236:239], a[252:255], v[228:231]\n"
    "v_mfma_f32_16x16x32_bf16 v[160:163], a[244:247], a[240:243], v[160:163]\n"
    "ds_write_b32 v105, v107 offset:1728\n"
    "v_mfma_f32_16x16x32_bf16 v[196:199], a[248:251], a[240:243], v[196:199]\n"
    "v_mfma_f32_16x16x32_bf16 v[232:235], a[240:243], a[252:255], v[232:235]\n"
    "s_waitcnt lgkmcnt(0)\n"
    "v_mfma_f32_16x16x32_bf16 v[128:131], a[196:199], a[160:163], v[128:131]\n"
    "s_waitcnt vmcnt(8)\n"
    "s_barrier\n"
    "ds_read_b64_tr_b16 a[244:245], v27 offset:36864\n"
    "v_mfma_f32_16x16x32_bf16 v[164:167], a[200:203], a[160:163], v[164:167]\n"
    "ds_read_b64_tr_b16 a[246:247], v27 offset:37376\n"
    "s_mov_b32 s50, 65536\n"
    "s_add_u32 m0, s59, 147456\n"
    "v_mfma_f32_16x16x32_bf16 v[200:203], a[160:163], a[204:207], v[200:203]\n"
    "ds_read_b64_tr_b16 a[248:249], v27 offset:37888\n"
    "v_mfma_f32_16x16x32_bf16 v[132:135], a[196:199], a[164:167], v[132:135]\n"
    "ds_read_b64_tr_b16 a[250:251], v27 offset:38400\n"
    "v_mfma_f32_16x16x32_bf16 v[168:171], a[200:203], a[164:167], v[168:171]\n"
    "ds_read_b64_tr_b16 a[252:253], v27 offset:38912\n"
    "v_mfma_f32_16x16x32_bf16 v[204:207], a[164:167], a[204:207], v[204:207]\n"
    "buffer_load_dwordx4 v2, s[12:15], s50 offen offset:0 lds\n"
    "ds_read_b64_tr_b16 a[254:255], v27 offset:39424\n"
    "v_mfma_f32_16x16x32_bf16 v[136:139], a[196:199], a[168:171], v[136:139]\n"
    "ds_read_b128 a[208:211], v26 offset:36864\n"
    "v_mfma_f32_16x16x32_bf16 v[172:175], a[200:203], a[168:171], v[172:175]\n"
    "ds_read_b128 a[212:215], v26 offset:20480\n"
    "v_mfma_f32_16x16x32_bf16 v[208:211], a[168:171], a[204:207], v[208:211]\n"
    "ds_read_b128 a[216:219], v26 offset:22528\n"
    "buffer_load_dwordx4 v3, s[12:15], s50 offen offset:1024 lds\n"
    "v_mfma_f32_16x16x32_bf16 v[140:143], a[196:199], a[172:175], v[140:143]\n"
    "ds_read_b128 a[220:223], v26 offset:24576\n"
    "v_mfma_f32_16x16x32_bf16 v[176:179], a[200:203], a[172:175], v[176:179]\n"
    "ds_read_b128 a[224:227], v26 offset:26624\n"
    "v_mfma_f32_16x16x32_bf16 v[212:215], a[172:175], a[204:207], v[212:215]\n"
    "ds_read_b128 a[228:231], v26 offset:28672\n"
    "v_mfma_f32_16x16x32_bf16 v[144:147], a[196:199], a[176:179], v[144:147]\n"
    "ds_read_b128 a[232:235], v26 offset:30720\n"
    "buffer_load_dwordx4 v4, s[12:15], s50 offen offset:2048 lds\n"
    "v_mfma_f32_16x16x32_bf16 v[180:183], a[200:203], a[176:179], v[180:183]\n"
    "ds_read_b128 a[236:239], v26 offset:32768\n"
    "v_mfma_f32_16x16x32_bf16 v[216:219], a[176:179], a[204:207], v[216:219]\n"
    "ds_read_b128 a[240:243], v26 offset:34816\n"
    "v_mfma_f32_16x16x32_bf16 v[148:151], a[196:199], a[180:183], v[148:151]\n"
    "v_mfma_f32_16x16x32_bf16 v[184:187], a[200:203], a[180:183], v[184:187]\n"
    "v_mfma_f32_16x16x32_bf16 v[220:223], a[180:183], a[204:207], v[220:223]\n"
    "buffer_load_dwordx4 v5, s[12:15], s50 offen offset:3072 lds\n"
    "v_mfma_f32_16x16x32_bf16 v[152:155], a[196:199], a[184:187], v[152:155]\n"
    "v_mfma_f32_16x16x32_bf16 v[188:191], a[200:203], a[184:187], v[188:191]\n"
    "v_mfma_f32_16x16x32_bf16 v[224:227], a[184:187], a[204:207], v[224:227]\n"
    "v_mfma_f32_16x16x32_bf16 v[156:159], a[196:199], a[188:191], v[156:159]\n"
    "v_mfma_f32_16x16x32_bf16 v[192:195], a[200:203], a[188:191], v[192:195]\n"
    "v_mfma_f32_16x16x32_bf16 v[228:231], a[188:191], a[204:207], v[228:231]\n"
    "v_mfma_f32_16x16x32_bf16 v[160:163], a[196:199], a[192:195], v[160:163]\n"
    "v_mfma_f32_16x16x32_bf16 v[196:199], a[200:203], a[192:195], v[196:199]\n"
    "v_mfma_f32_16x16x32_bf16 v[232:235], a[192:195], a[204:207], v[232:235]\n"
    "s_waitcnt lgkmcnt(0)\n"
    "v_mfma_f32_16x16x32_bf16 v[128:131], a[244:247], a[208:211], v[128:131]\n"
    "s_barrier\n"
    "ds_read_b64 v[236:237], v36 offset:0\n"
    "v_mfma_f32_16x16x32_bf16 v[164:167], a[248:251], a[208:211], v[164:167]\n"
    "ds_read_b64 v[238:239], v36 offset:128\n"
    "s_mov_b32 s50, 98304\n"
    "s_add_u32 m0, s59, 0\n"
    "v_mfma_f32_16x16x32_bf16 v[200:203], a[208:211], a[252:255], v[200:203]\n"
    "ds_read_b64 v[240:241], v36 offset:256\n"
    "v_mfma_f32_16x16x32_bf16 v[132:135], a[244:247], a[212:215], v[132:135]\n"
    "ds_read_b64 v[242:243], v36 offset:384\n"
    "v_mfma_f32_16x16x32_bf16 v[168:171], a[248:251], a[212:215], v[168:171]\n"
    "ds_read_b64 v[244:245], v36 offset:512\n"
    "v_mfma_f32_16x16x32_bf16 v[204:207], a[212:215], a[252:255], v[204:207]\n"
    "ds_read_b64 v[246:247], v36 offset:640\n"
    "v_mfma_f32_16x16x32_bf16 v[136:139], a[244:247], a[216:219], v[136:139]\n"
    "buffer_load_dwordx4 v2, s[12:15], s50 offen offset:0 lds\n"
    "ds_read_b64 v[248:249], v36 offset:768\n"
    "v_mfma_f32_16x16x32_bf16 v[172:175], a[248:251], a[216:219], v[172:175]\n"
    "ds_read_b64 v[250:251], v36 offset:896\n"
    "v_mfma_f32_16x16x32_bf16 v[208:211], a[216:219], a[252:255], v[208:211]\n"
    "ds_read_b64 v[252:253], v36 offset:1024\n"
    "v_mfma_f32_16x16x32_bf16 v[140:143], a[244:247], a[220:223], v[140:143]\n"
    "ds_read_b128 v[40:43], v37 offset:1152\n"
    "ds_read_b128 v[44:47], v37 offset:1728\n"
    "v_mfma_f32_16x16x32_bf16 v[176:179], a[248:251], a[220:223], v[176:179]\n"
    "ds_read_b128 v[48:51], v37 offset:1216\n"
    "buffer_load_dwordx4 v3, s[12:15], s50 offen offset:1024 lds\n"
    "ds_read_b128 v[52:55], v37 offset:1792\n"
    "v_mfma_f32_16x16x32_bf16 v[212:215], a[220:223], a[252:255], v[212:215]\n"
    "ds_read_b128 v[56:59], v37 offset:1280\n"
    "ds_read_b128 v[60:63], v37 offset:1856\n"
    "v_mfma_f32_16x16x32_bf16 v[144:147], a[244:247], a[224:227], v[144:147]\n"
    "ds_read_b128 v[64:67], v37 offset:1344\n"
    "ds_read_b128 v[68:71], v37 offset:1920\n"
    "v_mfma_f32_16x16x32_bf16 v[180:183], a[248:251], a[224:227], v[180:183]\n"
    "ds_read_b128 v[72:75], v37 offset:1408\n"
    "buffer_load_dwordx4 v4, s[12:15], s50 offen offset:2048 lds\n"
    "ds_read_b128 v[76:79], v37 offset:1984\n"
    "v_mfma_f32_16x16x32_bf16 v[216:219], a[224:227], a[252:255], v[216:219]\n"
    "ds_read_b128 v[80:83], v37 offset:1472\n"
    "ds_read_b128 v[84:87], v37 offset:2048\n"
    "v_mfma_f32_16x16x32_bf16 v[148:151], a[244:247], a[228:231], v[148:151]\n"
    "ds_read_b128 v[88:91], v37 offset:1536\n"
    "ds_read_b128 v[92:95], v37 offset:2112\n"
    "v_mfma_f32_16x16x32_bf16 v[184:187], a[248:251], a[228:231], v[184:187]\n"
    "ds_read_b128 v[96:99], v37 offset:1600\n"
    "ds_read_b128 v[100:103], v37 offset:2176\n"
    "buffer_load_dwordx4 v5, s[12:15], s50 offen offset:3072 lds\n"
    "v_mfma_f32_16x16x32_bf16 v[220:223], a[228:231], a[252:255], v[220:223]\n"
    "ds_read_b128 v[104:107], v37 offset:1664\n"
    "ds_read_b128 v[108:111], v37 offset:2240\n"
    "v_mfma_f32_16x16x32_bf16 v[152:155], a[244:247], a[232:235], v[152:155]\n"
    "v_mfma_f32_16x16x32_bf16 v[188:191], a[248:251], a[232:235], v[188:191]\n"
    "v_mfma_f32_16x16x32_bf16 v[224:227], a[232:235], a[252:255], v[224:227]\n"
    "v_mfma_f32_16x16x32_bf16 v[156:159], a[244:247], a[236:239], v[156:159]\n"
    "v_mfma_f32_16x16x32_bf16 v[192:195], a[248:251], a[236:239], v[192:195]\n"
    "v_mfma_f32_16x16x32_bf16 v[228:231], a[236:239], a[252:255], v[228:231]\n"
    "v_mfma_f32_16x16x32_bf16 v[160:163], a[244:247], a[240:243], v[160:163]\n"
    "v_mfma_f32_16x16x32_bf16 v[196:199], a[248:251], a[240:243], v[196:199]\n"
    "v_mfma_f32_16x16x32_bf16 v[232:235], a[240:243], a[252:255], v[232:235]\n"
    "s_waitcnt lgkmcnt(0)\n"
    "s_cmp_lt_u32 s52, 2\n"
    "s_cselect_b64 vcc, -1, 0\n"
    "v_mov_b32 v17, 0xff800000\n"
    "s_lshl_b32 s48, s53, 2\n"
    "s_add_u32 s48, s48, s3\n"
    "s_lshl_b32 s48, s48, 10\n"
    "v_cndmask_b32 v28, v112, v17, vcc\n"
    "v_cndmask_b32 v29, v113, v17, vcc\n"
    "v_cndmask_b32 v30, v114, v17, vcc\n"
    "v_cndmask_b32 v31, v115, v17, vcc\n"
    "v_mov_b32_dpp v18, v128 row_newbcast:0 row_mask:0xf bank_mask:0xf\n"
    "v_mov_b32_dpp v22, v164 row_newbcast:0 row_mask:0xf bank_mask:0xf\n"
    "s_waitcnt vmcnt(12)\n"
    "v_mov_b32_dpp v19, v129 row_newbcast:0 row_mask:0xf bank_mask:0xf\n"
    "v_mov_b32_dpp v23, v165 row_newbcast:0 row_mask:0xf bank_mask:0xf\n"
    "v_mov_b32_dpp v20, v130 row_newbcast:0 row_mask:0xf bank_mask:0xf\n"
    "v_mov_b32_dpp v24, v166 row_newbcast:0 row_mask:0xf bank_mask:0xf\n"
    "v_mov_b32_dpp v21, v131 row_newbcast:0 row_mask:0xf bank_mask:0xf\n"
    "v_mov_b32_dpp v25, v167 row_newbcast:0 row_mask:0xf bank_mask:0xf\n"
    "v_mov_b32 v26, v200\n"
    "v_mov_b32 v27, v200\n"
    "s_nop 1\n"
    "v_permlane32_swap_b32 v26, v27\n"
    "v_pk_fma_f32 v[200:201], v[40:41], v[26:27], v[200:201] op_sel_hi:[1,0,1]\n"
    "v_pk_fma_f32 v[202:203], v[42:43], v[26:27], v[202:203] op_sel_hi:[1,0,1]\n"
    "v_pk_mul_f32 v[200:201], v[200:201], v[44:45]\n"
    "v_pk_mul_f32 v[202:203], v[202:203], v[46:47]\n"
    "v_cvt_pk_bf16_f32 v200, v200, v201\n"
    "v_cvt_pk_bf16_f32 v201, v202, v203\n"
    "v_pk_fma_f32 v[204:205], v[48:49], v[26:27], v[204:205] op_sel_hi:[1,0,1]\n"
    "v_pk_fma_f32 v[206:207], v[50:51], v[26:27], v[206:207] op_sel_hi:[1,0,1]\n"
    "v_pk_mul_f32 v[204:205], v[204:205], v[52:53]\n"
    "ds_read_b64_tr_b16 a[0:1], v10 offset:16384\n"
    "ds_read_b64_tr_b16 a[2:3], v10 offset:18432\n"
    "v_pk_mul_f32 v[206:207], v[206:207], v[54:55]\n"
    "v_cvt_pk_bf16_f32 v202, v204, v205\n"
    "v_cvt_pk_bf16_f32 v203, v206, v207\n"
    "v_pk_fma_f32 v[208:209], v[56:57], v[26:27], v[208:209] op_sel_hi:[1,0,1]\n"
    "v_pk_fma_f32 v[210:211], v[58:59], v[26:27], v[210:211] op_sel_hi:[1,0,1]\n"
    "v_pk_mul_f32 v[208:209], v[208:209], v[60:61]\n"
    "v_pk_mul_f32 v[210:211], v[210:211], v[62:63]\n"
    "v_cvt_pk_bf16_f32 v204, v208, v209\n"
    "v_cvt_pk_bf16_f32 v205, v210, v211\n"
    "v_pk_fma_f32 v[212:213], v[64:65], v[26:27], v[212:213] op_sel_hi:[1,0,1]\n"
    "v_pk_fma_f32 v[214:215], v[66:67], v[26:27], v[214:215] op_sel_hi:[1,0,1]\n"
    "v_pk_mul_f32 v[212:213], v[212:213], v[68:69]\n"
    "v_pk_mul_f32 v[214:215], v[214:215], v[70:71]\n"
    "v_cvt_pk_bf16_f32 v206, v212, v213\n"
    "v_cvt_pk_bf16_f32 v207, v214, v215\n"
    "v_pk_fma_f32 v[216:217], v[72:73], v[26:27], v[216:217] op_sel_hi:[1,0,1]\n"
    "v_pk_fma_f32 v[218:219], v[74:75], v[26:27], v[218:219] op_sel_hi:[1,0,1]\n"
    "v_pk_mul_f32 v[216:217], v[216:217], v[76:77]\n"
    "ds_read_b64_tr_b16 a[4:5], v11 offset:16384\n"
    "ds_read_b64_tr_b16 a[6:7], v11 offset:18432\n"
    "v_pk_mul_f32 v[218:219], v[218:219], v[78:79]\n"
    "v_cvt_pk_bf16_f32 v208, v216, v217\n"
    "v_cvt_pk_bf16_f32 v209, v218, v219\n"
    "v_pk_fma_f32 v[220:221], v[80:81], v[26:27], v[220:221] op_sel_hi:[1,0,1]\n"
    "v_pk_fma_f32 v[222:223], v[82:83], v[26:27], v[222:223] op_sel_hi:[1,0,1]\n"
    "v_pk_mul_f32 v[220:221], v[220:221], v[84:85]\n"
    "v_pk_mul_f32 v[222:223], v[222:223], v[86:87]\n"
    "v_cvt_pk_bf16_f32 v210, v220, v221\n"
    "v_cvt_pk_bf16_f32 v211, v222, v223\n"
    "v_pk_fma_f32 v[224:225], v[88:89], v[26:27], v[224:225] op_sel_hi:[1,0,1]\n"
    "v_pk_fma_f32 v[226:227], v[90:91], v[26:27], v[226:227] op_sel_hi:[1,0,1]\n"
    "v_pk_mul_f32 v[224:225], v[224:225], v[92:93]\n"
    "v_pk_mul_f32 v[226:227], v[226:227], v[94:95]\n"
    "v_cvt_pk_bf16_f32 v212, v224, v225\n"
    "v_cvt_pk_bf16_f32 v213, v226, v227\n"
    "v_pk_fma_f32 v[228:229], v[96:97], v[26:27], v[228:229] op_sel_hi:[1,0,1]\n"
    "v_pk_fma_f32 v[230:231], v[98:99], v[26:27], v[230:231] op_sel_hi:[1,0,1]\n"
    "v_pk_mul_f32 v[228:229], v[228:229], v[100:101]\n"
    "ds_read_b64_tr_b16 a[8:9], v12 offset:16384\n"
    "ds_read_b64_tr_b16 a[10:11], v12 offset:18432\n"
    "v_pk_mul_f32 v[230:231], v[230:231], v[102:103]\n"
    "v_cvt_pk_bf16_f32 v214, v228, v229\n"
    "v_cvt_pk_bf16_f32 v215, v230, v231\n"
    "v_pk_fma_f32 v[232:233], v[104:105], v[26:27], v[232:233] op_sel_hi:[1,0,1]\n"
    "v_pk_fma_f32 v[234:235], v[106:107], v[26:27], v[234:235] op_sel_hi:[1,0,1]\n"
    "v_pk_mul_f32 v[232:233], v[232:233], v[108:109]\n"
    "v_pk_mul_f32 v[234:235], v[234:235], v[110:111]\n"
    "v_cvt_pk_bf16_f32 v216, v232, v233\n"
    "v_cvt_pk_bf16_f32 v217, v234, v235\n"
    "v_pk_fma_f32 v[164:165], v[22:23], v[236:237], v[164:165] op_sel_hi:[1,0,1]\n"
    "v_pk_fma_f32 v[166:167], v[24:25], v[236:237], v[166:167] op_sel_hi:[1,0,1]\n"
    "v_pk_mul_f32 v[164:165], v[164:165], v[236:237] op_sel:[0,1]\n"
    "v_pk_mul_f32 v[166:167], v[166:167], v[236:237] op_sel:[0,1]\n"
    "v_pk_fma_f32 v[168:169], v[22:23], v[238:239], v[168:169] op_sel_hi:[1,0,1]\n"
    "v_pk_fma_f32 v[170:171], v[24:25], v[238:239], v[170:171] op_sel_hi:[1,0,1]\n"
    "v_pk_fma_f32 v[132:133], v[18:19], v[238:239], v[132:133] op_sel_hi:[1,0,1]\n"
    "v_pk_fma_f32 v[134:135], v[20:21], v[238:239], v[134:135] op_sel_hi:[1,0,1]\n"
    "v_pk_mul_f32 v[168:169], v[168:169], v[238:239] op_sel:[0,1]\n"
    "ds_read_b64_tr_b16 a[12:13], v13 offset:16384\n"
    "ds_read_b64_tr_b16 a[14:15], v13 offset:18432\n"
    "v_pk_mul_f32 v[170:171], v[170:171], v[238:239] op_sel:[0,1]\n"
    "v_pk_fma_f32 v[172:173], v[22:23], v[240:241], v[172:173] op_sel_hi:[1,0,1]\n"
    "v_pk_fma_f32 v[174:175], v[24:25], v[240:241], v[174:175] op_sel_hi:[1,0,1]\n"
    "v_pk_fma_f32 v[136:137], v[18:19], v[240:241], v[136:137] op_sel_hi:[1,0,1]\n"
    "v_pk_fma_f32 v[138:139], v[20:21], v[240:241], v[138:139] op_sel_hi:[1,0,1]\n"
    "v_pk_mul_f32 v[172:173], v[172:173], v[240:241] op_sel:[0,1]\n"
    "v_pk_mul_f32 v[174:175], v[174:175], v[240:241] op_sel:[0,1]\n"
    "v_pk_fma_f32 v[176:177], v[22:23], v[242:243], v[176:177] op_sel_hi:[1,0,1]\n"
    "v_pk_fma_f32 v[178:179], v[24:25], v[242:243], v[178:179] op_sel_hi:[1,0,1]\n"
    "v_pk_fma_f32 v[140:141], v[18:19], v[242:243], v[140:141] op_sel_hi:[1,0,1]\n"
    "v_pk_fma_f32 v[142:143], v[20:21], v[242:243], v[142:143] op_sel_hi:[1,0,1]\n"
    "v_pk_mul_f32 v[176:177], v[176:177], v[242:243] op_sel:[0,1]\n"
    "v_pk_mul_f32 v[178:179], v[178:179], v[242:243] op_sel:[0,1]\n"
    "v_pk_fma_f32 v[180:181], v[22:23], v[244:245], v[180:181] op_sel_hi:[1,0,1]\n"
    "v_pk_fma_f32 v[182:183], v[24:25], v[244:245], v[182:183] op_sel_hi:[1,0,1]\n"
    "v_pk_fma_f32 v[144:145], v[18:19], v[244:245], v[144:145] op_sel_hi:[1,0,1]\n"
    "v_pk_fma_f32 v[146:147], v[20:21], v[244:245], v[146:147] op_sel_hi:[1,0,1]\n"
    "v_pk_mul_f32 v[180:181], v[180:181], v[244:245] op_sel:[0,1]\n"
    "s_mov_b32 s50, 131072\n"
    "s_add_u32 m0, s59, 49152\n"
    "v_pk_mul_f32 v[182:183], v[182:183], v[244:245] op_sel:[0,1]\n"
    "v_pk_fma_f32 v[184:185], v[22:23], v[246:247], v[184:185] op_sel_hi:[1,0,1]\n"
    "v_pk_fma_f32 v[186:187], v[24:25], v[246:247], v[186:187] op_sel_hi:[1,0,1]\n"
    "v_pk_fma_f32 v[148:149], v[18:19], v[246:247], v[148:149] op_sel_hi:[1,0,1]\n"
    "v_pk_fma_f32 v[150:151], v[20:21], v[246:247], v[150:151] op_sel_hi:[1,0,1]\n"
    "v_pk_mul_f32 v[184:185], v[184:185], v[246:247] op_sel:[0,1]\n"
    "v_pk_mul_f32 v[186:187], v[186:187], v[246:247] op_sel:[0,1]\n"
    "v_pk_fma_f32 v[188:189], v[22:23], v[248:249], v[188:189] op_sel_hi:[1,0,1]\n"
    "v_pk_fma_f32 v[190:191], v[24:25], v[248:249], v[190:191] op_sel_hi:[1,0,1]\n"
    "v_pk_fma_f32 v[152:153], v[18:19], v[248:249], v[152:153] op_sel_hi:[1,0,1]\n"
    "v_pk_fma_f32 v[154:155], v[20:21], v[248:249], v[154:155] op_sel_hi:[1,0,1]\n"
    "v_pk_mul_f32 v[188:189], v[188:189], v[248:249] op_sel:[0,1]\n"
    "v_pk_mul_f32 v[190:191], v[190:191], v[248:249] op_sel:[0,1]\n"
    "v_pk_fma_f32 v[192:193], v[22:23], v[250:251], v[192:193] op_sel_hi:[1,0,1]\n"
    "v_pk_fma_f32 v[194:195], v[24:25], v[250:251], v[194:195] op_sel_hi:[1,0,1]\n"
    "v_pk_fma_f32 v[156:157], v[18:19], v[250:251], v[156:157] op_sel_hi:[1,0,1]\n"
    "v_pk_fma_f32 v[158:159], v[20:21], v[250:251], v[158:159] op_sel_hi:[1,0,1]\n"
    "v_pk_mul_f32 v[192:193], v[192:193], v[250:251] op_sel:[0,1]\n"
    "buffer_load_dwordx4 v2, s[12:15], s50 offen offset:0 lds\n"
    "v_pk_mul_f32 v[194:195], v[194:195], v[250:251] op_sel:[0,1]\n"
    "v_pk_fma_f32 v[196:197], v[22:23], v[252:253], v[196:197] op_sel_hi:[1,0,1]\n"
    "v_pk_fma_f32 v[198:199], v[24:25], v[252:253], v[198:199] op_sel_hi:[1,0,1]\n"
    "v_pk_fma_f32 v[160:161], v[18:19], v[252:253], v[160:161] op_sel_hi:[1,0,1]\n"
    "v_pk_fma_f32 v[162:163], v[20:21], v[252:253], v[162:163] op_sel_hi:[1,0,1]\n"
    "v_pk_mul_f32 v[196:197], v[196:197], v[252:253] op_sel:[0,1]\n"
    "v_pk_mul_f32 v[198:199], v[198:199], v[252:253] op_sel:[0,1]\n"
    "v_cvt_pk_bf16_f32 v164, v164, v165\n"
    "v_cvt_pk_bf16_f32 v165, v166, v167\n"
    "v_cvt_pk_bf16_f32 v168, v168, v169\n"
    "v_cvt_pk_bf16_f32 v169, v170, v171\n"
    "v_cvt_pk_bf16_f32 v132, v132, v133\n"
    "v_cvt_pk_bf16_f32 v133, v134, v135\n"
    "v_mul_f32 v218, 0x3eb8aa3b, v239\n"
    "v_cvt_pk_bf16_f32 v172, v172, v173\n"
    "v_cvt_pk_bf16_f32 v173, v174, v175\n"
    "v_cvt_pk_bf16_f32 v136, v136, v137\n"
    "v_cvt_pk_bf16_f32 v137, v138, v139\n"
    "buffer_load_dwordx4 v3, s[12:15], s50 offen offset:1024 lds\n"
    "v_mul_f32 v219, 0x3eb8aa3b, v241\n"
    "v_mfma_f32_16x16x16_bf16 v[40:43], v[164:165], v[132:133], v[28:31]\n"
    "v_mfma_f32_16x16x16_bf16 v[44:47], v[168:169], v[132:133], v[116:119]\n"
    "v_cvt_pk_bf16_f32 v176, v176, v177\n"
    "v_cvt_pk_bf16_f32 v177, v178, v179\n"
    "v_cvt_pk_bf16_f32 v140, v140, v141\n"
    "v_cvt_pk_bf16_f32 v141, v142, v143\n"
    "v_mul_f32 v220, 0x3eb8aa3b, v243\n"
    "v_mfma_f32_16x16x16_bf16 v[48:51], v[168:169], v[136:137], v[112:115]\n"
    "v_mfma_f32_16x16x16_bf16 v[52:55], v[172:173], v[136:137], v[116:119]\n"
    "v_cvt_pk_bf16_f32 v180, v180, v181\n"
    "v_cvt_pk_bf16_f32 v181, v182, v183\n"
    "v_cvt_pk_bf16_f32 v144, v144, v145\n"
    "v_cvt_pk_bf16_f32 v145, v146, v147\n"
    "v_mul_f32 v221, 0x3eb8aa3b, v245\n"
    "v_mfma_f32_16x16x16_bf16 v[56:59], v[172:173], v[140:141], v[112:115]\n"
    "v_mfma_f32_16x16x16_bf16 v[60:63], v[176:177], v[140:141], v[116:119]\n"
    "v_cvt_pk_bf16_f32 v184, v184, v185\n"
    "buffer_load_dwordx4 v4, s[12:15], s50 offen offset:2048 lds\n"
    "v_cvt_pk_bf16_f32 v185, v186, v187\n"
    "v_cvt_pk_bf16_f32 v148, v148, v149\n"
    "v_cvt_pk_bf16_f32 v149, v150, v151\n"
    "v_mul_f32 v222, 0x3eb8aa3b, v247\n"
    "v_mfma_f32_16x16x16_bf16 v[64:67], v[176:177], v[144:145], v[112:115]\n"
    "v_mfma_f32_16x16x16_bf16 v[68:71], v[180:181], v[144:145], v[116:119]\n"
    "v_cvt_pk_bf16_f32 v188, v188, v189\n"
    "v_cvt_pk_bf16_f32 v189, v190, v191\n"
    "v_cvt_pk_bf16_f32 v152, v152, v153\n"
    "v_cvt_pk_bf16_f32 v153, v154, v155\n"
    "v_mul_f32 v223, 0x3eb8aa3b, v249\n"
    "v_mfma_f32_16x16x16_bf16 v[72:75], v[180:181], v[148:149], v[112:115]\n"
    "v_mfma_f32_16x16x16_bf16 v[76:79], v[184:185], v[148:149], v[116:119]\n"
    "v_cvt_pk_bf16_f32 v192, v192, v193\n"
    "v_cvt_pk_bf16_f32 v193, v194, v195\n"
    "v_cvt_pk_bf16_f32 v156, v156, v157\n"
    "v_cvt_pk_bf16_f32 v157, v158, v159\n"
    "v_mul_f32 v224, 0x3eb8aa3b, v251\n"
    "buffer_load_dwordx4 v5, s[12:15], s50 offen offset:3072 lds\n"
    "v_mfma_f32_16x16x16_bf16 v[80:83], v[184:185], v[152:153], v[112:115]\n"
    "v_mfma_f32_16x16x16_bf16 v[84:87], v[188:189], v[152:153], v[116:119]\n"
    "v_cvt_pk_bf16_f32 v196, v196, v197\n"
    "v_cvt_pk_bf16_f32 v197, v198, v199\n"
    "v_cvt_pk_bf16_f32 v160, v160, v161\n"
    "v_cvt_pk_bf16_f32 v161, v162, v163\n"
    "v_mul_f32 v225, 0x3eb8aa3b, v253\n"
    "v_mfma_f32_16x16x16_bf16 v[88:91], v[188:189], v[156:157], v[112:115]\n"
    "v_mfma_f32_16x16x16_bf16 v[92:95], v[192:193], v[156:157], v[116:119]\n"
    "v_mfma_f32_16x16x16_bf16 v[96:99], v[192:193], v[160:161], v[112:115]\n"
    "v_mfma_f32_16x16x16_bf16 v[100:103], v[196:197], v[160:161], v[116:119]\n"
    "v_max_f32 v40, v40, v44\n"
    "v_max_f32 v41, v41, v45\n"
    "v_max_f32 v42, v42, v46\n"
    "v_max_f32 v43, v43, v47\n"
    "v_max3_f32 v226, v40, v41, v42\n"
    "v_max_f32 v226, v226, v43\n"
    "v_max_f32 v48, v48, v52\n"
    "s_waitcnt vmcnt(12)\n"
    "v_max_f32 v49, v49, v53\n"
    "v_max_f32 v50, v50, v54\n"
    "v_max_f32 v51, v51, v55\n"
    "v_max3_f32 v227, v48, v49, v50\n"
    "v_max_f32 v227, v227, v51\n"
    "v_max_f32 v56, v56, v60\n"
    "v_max_f32 v57, v57, v61\n"
    "v_max_f32 v58, v58, v62\n"
    "v_max_f32 v59, v59, v63\n"
    "v_max3_f32 v228, v56, v57, v58\n"
    "v_max_f32 v228, v228, v59\n"
    "v_max_f32 v64, v64, v68\n"
    "v_max_f32 v65, v65, v69\n"
    "v_max_f32 v66, v66, v70\n"
    "v_max_f32 v67, v67, v71\n"
    "v_max3_f32 v229, v64, v65, v66\n"
    "v_max_f32 v229, v229, v67\n"
    "v_max_f32 v72, v72, v76\n"
    "ds_read_b64_tr_b16 a[16:17], v10 offset:32768\n"
    "ds_read_b64_tr_b16 a[18:19], v10 offset:34816\n"
    "v_max_f32 v73, v73, v77\n"
    "v_max_f32 v74, v74, v78\n"
    "v_max_f32 v75, v75, v79\n"
    "v_max3_f32 v230, v72, v73, v74\n"
    "v_max_f32 v230, v230, v75\n"
    "v_max_f32 v80, v80, v84\n"
    "v_max_f32 v81, v81, v85\n"
    "v_max_f32 v82, v82, v86\n"
    "v_max_f32 v83, v83, v87\n"
    "v_max3_f32 v231, v80, v81, v82\n"
    "v_max_f32 v231, v231, v83\n"
    "v_max_f32 v88, v88, v92\n"
    "v_max_f32 v89, v89, v93\n"
    "v_max_f32 v90, v90, v94\n"
    "v_max_f32 v91, v91, v95\n"
    "v_max3_f32 v232, v88, v89, v90\n"
    "v_max_f32 v232, v232, v91\n"
    "v_max_f32 v96, v96, v100\n"
    "ds_read_b64_tr_b16 a[20:21], v11 offset:32768\n"
    "ds_read_b64_tr_b16 a[22:23], v11 offset:34816\n"
    "v_max_f32 v97, v97, v101\n"
    "v_max_f32 v98, v98, v102\n"
    "v_max_f32 v99, v99, v103\n"
    "v_max3_f32 v233, v96, v97, v98\n"
    "v_max_f32 v233, v233, v99\n"
    "v_permlane32_swap_b32 v226, v227\n"
    "v_permlane32_swap_b32 v230, v231\n"
    "v_permlane32_swap_b32 v228, v229\n"
    "v_permlane32_swap_b32 v232, v233\n"
    "v_max_f32 v226, v226, v227\n"
    "v_max_f32 v230, v230, v231\n"
    "v_max_f32 v228, v228, v229\n"
    "v_max_f32 v232, v232, v233\n"
    "s_nop 0\n"
    "v_permlane16_swap_b32 v226, v228\n"
    "v_permlane16_swap_b32 v230, v232\n"
    "v_max_f32 v226, v226, v228\n"
    "v_max_f32 v230, v230, v232\n"
    "v_mov_b32 v228, v226\n"
    "ds_read_b64_tr_b16 a[24:25], v12 offset:32768\n"
    "ds_read_b64_tr_b16 a[26:27], v12 offset:34816\n"
    "v_mov_b32 v232, v230\n"
    "v_permlane16_swap_b32 v226, v228\n"
    "s_nop 0\n"
    "v_permlane16_swap_b32 v230, v232\n"
    "v_mov_b32 v227, v226\n"
    "v_mov_b32 v231, v230\n"
    "v_mov_b32 v229, v228\n"
    "v_mov_b32 v233, v232\n"
    "v_permlane32_swap_b32 v226, v227\n"
    "v_permlane32_swap_b32 v230, v231\n"
    "v_permlane32_swap_b32 v228, v229\n"
    "v_permlane32_swap_b32 v232, v233\n"
    "v_mul_f32_e64 v226, -v226, v218\n"
    "v_mul_f32_e64 v227, -v227, v219\n"
    "v_mul_f32_e64 v228, -v228, v220\n"
    "v_mul_f32_e64 v229, -v229, v221\n"
    "v_mul_f32_e64 v230, -v230, v222\n"
    "v_mul_f32_e64 v231, -v231, v223\n"
    "v_mul_f32_e64 v232, -v232, v224\n"
    "ds_read_b64_tr_b16 a[28:29], v13 offset:32768\n"
    "ds_read_b64_tr_b16 a[30:31], v13 offset:34816\n"
    "v_mul_f32_e64 v233, -v233, v225\n"
    "v_fma_f32 v40, v40, v218, v226\n"
    "v_fma_f32 v41, v41, v218, v226\n"
    "v_fma_f32 v42, v42, v218, v226\n"
    "v_fma_f32 v43, v43, v218, v226\n"
    "v_exp_f32 v40, v40\n"
    "v_exp_f32 v41, v41\n"
    "v_exp_f32 v42, v42\n"
    "v_exp_f32 v43, v43\n"
    "v_cvt_pk_bf16_f32 v46, v40, v41\n"
    "v_cvt_pk_bf16_f32 v47, v42, v43\n"
    "v_and_b32 v44, v46, v124\n"
    "v_and_b32 v45, v47, v125\n"
    "v_xor_b32 v46, v46, v44\n"
    "v_xor_b32 v47, v47, v45\n"
    "v_fma_f32 v48, v48, v219, v227\n"
    "v_fma_f32 v49, v49, v219, v227\n"
    "v_fma_f32 v50, v50, v219, v227\n"
    "s_mov_b32 s50, 163840\n"
    "s_add_u32 m0, s59, 16384\n"
    "v_fma_f32 v51, v51, v219, v227\n"
    "v_exp_f32 v48, v48\n"
    "v_exp_f32 v49, v49\n"
    "v_exp_f32 v50, v50\n"
    "v_exp_f32 v51, v51\n"
    "v_mfma_f32_16x16x32_bf16 v[40:43], v[200:203], v[44:47], 0\n"
    "v_mfma_f32_16x16x32_bf16 v[132:135], v[120:123], v[44:47], 0\n"
    "v_cvt_pk_bf16_f32 v54, v48, v49\n"
    "v_cvt_pk_bf16_f32 v55, v50, v51\n"
    "v_and_b32 v52, v54, v124\n"
    "v_and_b32 v53, v55, v125\n"
    "v_xor_b32 v54, v54, v52\n"
    "v_xor_b32 v55, v55, v53\n"
    "v_fma_f32 v56, v56, v220, v228\n"
    "v_fma_f32 v57, v57, v220, v228\n"
    "v_fma_f32 v58, v58, v220, v228\n"
    "v_fma_f32 v59, v59, v220, v228\n"
    "v_exp_f32 v56, v56\n"
    "buffer_load_dwordx4 v2, s[12:15], s50 offen offset:0 lds\n"
    "v_exp_f32 v57, v57\n"
    "v_exp_f32 v58, v58\n"
    "v_exp_f32 v59, v59\n"
    "v_mfma_f32_16x16x32_bf16 v[48:51], v[202:205], v[52:55], 0\n"
    "v_mfma_f32_16x16x32_bf16 v[136:139], v[120:123], v[52:55], 0\n"
    "v_cvt_pk_bf16_f32 v62, v56, v57\n"
    "v_cvt_pk_bf16_f32 v63, v58, v59\n"
    "v_and_b32 v60, v62, v124\n"
    "v_and_b32 v61, v63, v125\n"
    "v_xor_b32 v62, v62, v60\n"
    "v_xor_b32 v63, v63, v61\n"
    "v_fma_f32 v64, v64, v221, v229\n"
    "v_fma_f32 v65, v65, v221, v229\n"
    "v_fma_f32 v66, v66, v221, v229\n"
    "v_fma_f32 v67, v67, v221, v229\n"
    "v_exp_f32 v64, v64\n"
    "v_exp_f32 v65, v65\n"
    "v_exp_f32 v66, v66\n"
    "buffer_load_dwordx4 v3, s[12:15], s50 offen offset:1024 lds\n"
    "v_exp_f32 v67, v67\n"
    "v_mfma_f32_16x16x32_bf16 v[56:59], v[204:207], v[60:63], 0\n"
    "v_mfma_f32_16x16x32_bf16 v[140:143], v[120:123], v[60:63], 0\n"
    "v_cvt_pk_bf16_f32 v70, v64, v65\n"
    "v_cvt_pk_bf16_f32 v71, v66, v67\n"
    "v_and_b32 v68, v70, v124\n"
    "v_and_b32 v69, v71, v125\n"
    "v_xor_b32 v70, v70, v68\n"
    "v_xor_b32 v71, v71, v69\n"
    "v_fma_f32 v72, v72, v222, v230\n"
    "v_fma_f32 v73, v73, v222, v230\n"
    "v_fma_f32 v74, v74, v222, v230\n"
    "v_fma_f32 v75, v75, v222, v230\n"
    "v_exp_f32 v72, v72\n"
    "v_exp_f32 v73, v73\n"
    "v_exp_f32 v74, v74\n"
    "v_exp_f32 v75, v75\n"
    "v_mfma_f32_16x16x32_bf16 v[64:67], v[206:209], v[68:71], 0\n"
    "buffer_load_dwordx4 v4, s[12:15], s50 offen offset:2048 lds\n"
    "v_mfma_f32_16x16x32_bf16 v[144:147], v[120:123], v[68:71], 0\n"
    "v_cvt_pk_bf16_f32 v78, v72, v73\n"
    "v_cvt_pk_bf16_f32 v79, v74, v75\n"
    "v_and_b32 v76, v78, v124\n"
    "v_and_b32 v77, v79, v125\n"
    "v_xor_b32 v78, v78, v76\n"
    "v_xor_b32 v79, v79, v77\n"
    "v_fma_f32 v80, v80, v223, v231\n"
    "v_fma_f32 v81, v81, v223, v231\n"
    "v_fma_f32 v82, v82, v223, v231\n"
    "v_fma_f32 v83, v83, v223, v231\n"
    "v_exp_f32 v80, v80\n"
    "v_exp_f32 v81, v81\n"
    "v_exp_f32 v82, v82\n"
    "v_exp_f32 v83, v83\n"
    "v_mfma_f32_16x16x32_bf16 v[72:75], v[208:211], v[76:79], 0\n"
    "v_mfma_f32_16x16x32_bf16 v[148:151], v[120:123], v[76:79], 0\n"
    "v_cvt_pk_bf16_f32 v86, v80, v81\n"
    "buffer_load_dwordx4 v5, s[12:15], s50 offen offset:3072 lds\n"
    "v_cvt_pk_bf16_f32 v87, v82, v83\n"
    "v_and_b32 v84, v86, v124\n"
    "v_and_b32 v85, v87, v125\n"
    "v_xor_b32 v86, v86, v84\n"
    "v_xor_b32 v87, v87, v85\n"
    "v_fma_f32 v88, v88, v224, v232\n"
    "v_fma_f32 v89, v89, v224, v232\n"
    "v_fma_f32 v90, v90, v224, v232\n"
    "v_fma_f32 v91, v91, v224, v232\n"
    "v_exp_f32 v88, v88\n"
    "v_exp_f32 v89, v89\n"
    "v_exp_f32 v90, v90\n"
    "v_exp_f32 v91, v91\n"
    "v_mfma_f32_16x16x32_bf16 v[80:83], v[210:213], v[84:87], 0\n"
    "v_mfma_f32_16x16x32_bf16 v[152:155], v[120:123], v[84:87], 0\n"
    "v_cvt_pk_bf16_f32 v94, v88, v89\n"
    "v_cvt_pk_bf16_f32 v95, v90, v91\n"
    "v_and_b32 v92, v94, v124\n"
    "s_waitcnt vmcnt(12)\n"
    "v_and_b32 v93, v95, v125\n"
    "v_xor_b32 v94, v94, v92\n"
    "v_xor_b32 v95, v95, v93\n"
    "v_fma_f32 v96, v96, v225, v233\n"
    "v_fma_f32 v97, v97, v225, v233\n"
    "v_fma_f32 v98, v98, v225, v233\n"
    "v_fma_f32 v99, v99, v225, v233\n"
    "v_exp_f32 v96, v96\n"
    "v_exp_f32 v97, v97\n"
    "v_exp_f32 v98, v98\n"
    "v_exp_f32 v99, v99\n"
    "v_mfma_f32_16x16x32_bf16 v[88:91], v[212:215], v[92:95], 0\n"
    "v_mfma_f32_16x16x32_bf16 v[156:159], v[120:123], v[92:95], 0\n"
    "v_cvt_pk_bf16_f32 v102, v96, v97\n"
    "v_cvt_pk_bf16_f32 v103, v98, v99\n"
    "v_and_b32 v100, v102, v124\n"
    "v_and_b32 v101, v103, v125\n"
    "v_xor_b32 v102, v102, v100\n"
    "ds_read_b64_tr_b16 a[32:33], v10 offset:49152\n"
    "ds_read_b64_tr_b16 a[34:35], v10 offset:51200\n"
    "v_xor_b32 v103, v103, v101\n"
    "s_nop 1\n"
    "v_mfma_f32_16x16x32_bf16 v[96:99], v[214:217], v[100:103], 0\n"
    "v_mfma_f32_16x16x32_bf16 v[160:163], v[120:123], v[100:103], 0\n"
    "v_rcp_f32 v132, v132\n"
    "v_rcp_f32 v136, v136\n"
    "v_mul_f32 v40, v40, v132\n"
    "v_mul_f32 v41, v41, v132\n"
    "v_mul_f32 v42, v42, v132\n"
    "v_mul_f32 v43, v43, v132\n"
    "v_mul_f32 v48, v48, v136\n"
    "v_mul_f32 v49, v49, v136\n"
    "v_mul_f32 v50, v50, v136\n"
    "v_mul_f32 v51, v51, v136\n"
    "v_cvt_pk_bf16_f32 v164, v40, v41\n"
    "v_cvt_pk_bf16_f32 v165, v42, v43\n"
    "v_cvt_pk_bf16_f32 v166, v48, v49\n"
    "v_cvt_pk_bf16_f32 v167, v50, v51\n"
    "s_add_u32 s49, s48, 0\n"
    "ds_read_b64_tr_b16 a[36:37], v11 offset:49152\n"
    "ds_read_b64_tr_b16 a[38:39], v11 offset:51200\n"
    "s_add_u32 s49, s49, s64\n"
    "buffer_store_dwordx4 v[164:167], v14, s[16:19], s49 offen sc1\n"
    "v_rcp_f32 v140, v140\n"
    "v_rcp_f32 v144, v144\n"
    "v_mul_f32 v56, v56, v140\n"
    "v_mul_f32 v57, v57, v140\n"
    "v_mul_f32 v58, v58, v140\n"
    "v_mul_f32 v59, v59, v140\n"
    "v_mul_f32 v64, v64, v144\n"
    "v_mul_f32 v65, v65, v144\n"
    "v_mul_f32 v66, v66, v144\n"
    "v_mul_f32 v67, v67, v144\n"
    "v_cvt_pk_bf16_f32 v168, v56, v57\n"
    "v_cvt_pk_bf16_f32 v169, v58, v59\n"
    "v_cvt_pk_bf16_f32 v170, v64, v65\n"
    "v_cvt_pk_bf16_f32 v171, v66, v67\n"
    "s_add_u32 s49, s48, 147456\n"
    "s_add_u32 s49, s49, s64\n"
    "ds_read_b64_tr_b16 a[40:41], v12 offset:49152\n"
    "ds_read_b64_tr_b16 a[42:43], v12 offset:51200\n"
    "buffer_store_dwordx4 v[168:171], v14, s[16:19], s49 offen sc1\n"
    "v_rcp_f32 v148, v148\n"
    "v_rcp_f32 v152, v152\n"
    "v_mul_f32 v72, v72, v148\n"
    "v_mul_f32 v73, v73, v148\n"
    "v_mul_f32 v74, v74, v148\n"
    "v_mul_f32 v75, v75, v148\n"
    "v_mul_f32 v80, v80, v152\n"
    "v_mul_f32 v81, v81, v152\n"
    "v_mul_f32 v82, v82, v152\n"
    "v_mul_f32 v83, v83, v152\n"
    "v_cvt_pk_bf16_f32 v172, v72, v73\n"
    "v_cvt_pk_bf16_f32 v173, v74, v75\n"
    "v_cvt_pk_bf16_f32 v174, v80, v81\n"
    "v_cvt_pk_bf16_f32 v175, v82, v83\n"
    "s_add_u32 s49, s48, 294912\n"
    "s_add_u32 s49, s49, s64\n"
    "buffer_store_dwordx4 v[172:175], v14, s[16:19], s49 offen sc1\n"
    "ds_read_b64_tr_b16 a[44:45], v13 offset:49152\n"
    "ds_read_b64_tr_b16 a[46:47], v13 offset:51200\n"
    "v_rcp_f32 v156, v156\n"
    "v_rcp_f32 v160, v160\n"
    "v_mul_f32 v88, v88, v156\n"
    "v_mul_f32 v89, v89, v156\n"
    "v_mul_f32 v90, v90, v156\n"
    "v_mul_f32 v91, v91, v156\n"
    "v_mul_f32 v96, v96, v160\n"
    "v_mul_f32 v97, v97, v160\n"
    "v_mul_f32 v98, v98, v160\n"
    "v_mul_f32 v99, v99, v160\n"
    "v_cvt_pk_bf16_f32 v176, v88, v89\n"
    "v_cvt_pk_bf16_f32 v177, v90, v91\n"
    "v_cvt_pk_bf16_f32 v178, v96, v97\n"
    "v_cvt_pk_bf16_f32 v179, v98, v99\n"
    "s_add_u32 s49, s48, 442368\n"
    "s_add_u32 s49, s49, s64\n"
    "buffer_store_dwordx4 v[176:179], v14, s[16:19], s49 offen sc1\n"
    "s_mov_b32 s50, 196608\n"
    "s_add_u32 m0, s59, 32768\n"
    "s_nop 0\n"
    "buffer_load_dwordx4 v2, s[12:15], s50 offen offset:0 lds\n"
    "buffer_load_dwordx4 v3, s[12:15], s50 offen offset:1024 lds\n"
    "buffer_load_dwordx4 v4, s[12:15], s50 offen offset:2048 lds\n"
    "buffer_load_dwordx4 v5, s[12:15], s50 offen offset:3072 lds\n"
    "s_waitcnt vmcnt(16)\n"
    "ds_read_b64_tr_b16 a[48:49], v6 offset:0\n"
    "ds_read_b64_tr_b16 a[50:51], v6 offset:2048\n"
    "ds_read_b64_tr_b16 a[52:53], v7 offset:0\n"
    "ds_read_b64_tr_b16 a[54:55], v7 offset:2048\n"
    "ds_read_b64_tr_b16 a[56:57], v8 offset:0\n"
    "ds_read_b64_tr_b16 a[58:59], v8 offset:2048\n"
    "ds_read_b64_tr_b16 a[60:61], v9 offset:0\n"
    "ds_read_b64_tr_b16 a[62:63], v9 offset:2048\n"
    "s_waitcnt lgkmcnt(15)\n"
    "s_mov_b32 s50, 229376\n"
    "s_add_u32 m0, s59, 114688\n"
    "s_nop 0\n"
    "buffer_load_dwordx4 v2, s[12:15], s50 offen offset:0 lds\n"
    "buffer_load_dwordx4 v3, s[12:15], s50 offen offset:1024 lds\n"
    "buffer_load_dwordx4 v4, s[12:15], s50 offen offset:2048 lds\n"
    "buffer_load_dwordx4 v5, s[12:15], s50 offen offset:3072 lds\n"
    "v_mov_b32 v40, s66\n"
    "v_mov_b32 v41, s67\n"
    "s_waitcnt vmcnt(8)\n"
    "s_barrier\n"
    "buffer_store_dwordx2 v[40:41], v15, s[16:19], 0 offen sc1\n"
    "s_branch L_join\n"
    "L_ffn:\n"
    "s_and_b32 s48, s3, 1\n"
    "s_lshr_b32 s49, s3, 1\n"
    "s_lshl_b32 s50, s49, 13\n"
    "v_add_u32 v25, s50, v25\n"
    "s_lshl_b32 s50, s49, 9\n"
    "s_add_u32 s50, s50, 128\n"
    "v_add_u32 v36, s50, v36\n"
    "s_mul_i32 s26, s48, 6\n"
    "s_mul_i32 s50, s49, 3\n"
    "s_sub_u32 s27, s26, s50\n"
    "s_add_u32 s27, s27, 3\n"
    "s_add_u32 s26, s26, s50\n"
    "s_add_u32 s50, s26, 0\n"
    "s_and_b32 s51, s50, 3\n"
    "s_lshl_b32 s51, s51, 5\n"
    "v_xor_b32 v27, s51, v126\n"
    "s_lshr_b32 s51, s50, 2\n"
    "s_lshl_b32 s51, s51, 10\n"
    "v_add_u32 v27, s51, v27\n"
    "s_add_u32 s50, s26, 1\n"
    "s_and_b32 s51, s50, 3\n"
    "s_lshl_b32 s51, s51, 5\n"
    "v_xor_b32 v28, s51, v126\n"
    "s_lshr_b32 s51, s50, 2\n"
    "s_lshl_b32 s51, s51, 10\n"
    "v_add_u32 v28, s51, v28\n"
    "s_add_u32 s50, s26, 2\n"
    "s_and_b32 s51, s50, 3\n"
    "s_lshl_b32 s51, s51, 5\n"
    "v_xor_b32 v29, s51, v126\n"
    "s_lshr_b32 s51, s50, 2\n"
    "s_lshl_b32 s51, s51, 10\n"
    "v_add_u32 v29, s51, v29\n"
    "s_add_u32 s50, s27, 0\n"
    "s_and_b32 s51, s50, 3\n"
    "s_lshl_b32 s51, s51, 5\n"
    "v_xor_b32 v31, s51, v126\n"
    "s_lshr_b32 s51, s50, 2\n"
    "s_lshl_b32 s51, s51, 10\n"
    "v_add_u32 v31, s51, v31\n"
    "s_add_u32 s50, s27, 1\n"
    "s_and_b32 s51, s50, 3\n"
    "s_lshl_b32 s51, s51, 5\n"
    "v_xor_b32 v32, s51, v126\n"
    "s_lshr_b32 s51, s50, 2\n"
    "s_lshl_b32 s51, s51, 10\n"
    "v_add_u32 v32, s51, v32\n"
    "s_add_u32 s50, s27, 2\n"
    "s_and_b32 s51, s50, 3\n"
    "s_lshl_b32 s51, s51, 5\n"
    "v_xor_b32 v33, s51, v126\n"
    "s_lshr_b32 s51, s50, 2\n"
    "s_lshl_b32 s51, s51, 10\n"
    "v_add_u32 v33, s51, v33\n"
    "v_lshrrev_b32 v40, 4, v1\n"
    "v_and_b32 v41, 1, v40\n"
    "v_xor_b32 v41, s48, v41\n"
    "v_lshlrev_b32 v41, 5, v41\n"
    "v_lshl_add_u32 v41, v40, 9, v41\n"
    "v_bfe_u32 v42, v1, 2, 2\n"
    "v_lshl_add_u32 v41, v42, 6, v41\n"
    "v_and_b32 v42, 3, v1\n"
    "v_lshl_add_u32 v41, v42, 3, v41\n"
    "v_add_u32 v30, 65536, v41\n"
    "s_bitcmp1_b32 s53, 0\n"
    "s_cselect_b32 s50, 2, 0\n"
    "s_cselect_b32 s51, 0, 12\n"
    "s_add_u32 s26, s26, s50\n"
    "s_add_u32 s27, s27, s50\n"
    "s_add_u32 s30, s51, s48\n"
    "v_lshrrev_b32 v41, 1, v1\n"
    "v_lshrrev_b32 v42, 5, v1\n"
    "v_xor_b32 v41, v41, v42\n"
    "v_and_b32 v41, 1, v41\n"
    "v_cmp_eq_u32 vcc, 0, v41\n"
    "v_mov_b32 v42, 0x3f803f80\n"
    "s_nop 0\n"
    "v_cndmask_b32 v248, 0, v42, vcc\n"
    "v_and_b32 v41, 1, v1\n"
    "v_mul_u32_u24 v41, 0xaaaa, v41\n"
    "v_add_u32 v252, 0x4444, v41\n"
    "v_mov_b32 v249, v248\n"
    "v_mov_b32 v250, v248\n"
    "v_mov_b32 v251, v248\n"
    "v_mov_b32 v240, 0\n"
    "v_mov_b32 v241, 0\n"
    "v_mov_b32 v242, 0\n"
    "v_mov_b32 v243, 0\n"
    "v_mov_b32 v244, 0\n"
    "v_mov_b32 v245, 0\n"
    "v_mov_b32 v246, 0\n"
    "v_mov_b32 v247, 0\n"
    "v_and_b32 v41, 15, v1\n"
    "v_lshlrev_b32 v41, 2, v41\n"
    "v_add_u32 v38, 112896, v41\n"
    "v_lshlrev_b32 v124, 3, v40\n"
    "s_mov_b64 s[20:21], s[32:33]\n"
    "s_mov_b32 s22, 3584\n"
    "v_xor_b32 v26, 64, v25\n"
    "s_waitcnt vmcnt(14) lgkmcnt(0)\n"
    "s_barrier\n"
    "ds_read_b128 a[160:163], v25 offset:0\n"
    "ds_read_b128 a[164:167], v25 offset:2048\n"
    "ds_read_b128 a[168:171], v25 offset:4096\n"
    "ds_read_b128 a[172:175], v25 offset:6144\n"
    "ds_read_b64_tr_b16 a[176:177], v27 offset:0\n"
    "ds_read_b64_tr_b16 a[178:179], v27 offset:512\n"
    "ds_read_b64_tr_b16 a[180:181], v28 offset:0\n"
    "ds_read_b64_tr_b16 a[182:183], v28 offset:512\n"
    "ds_read_b64_tr_b16 a[184:185], v29 offset:0\n"
    "ds_read_b64_tr_b16 a[186:187], v29 offset:512\n"
    "ds_read_b64_tr_b16 a[188:189], v30 offset:0\n"
    "ds_read_b64_tr_b16 a[190:191], v30 offset:256\n"
    "ds_read_b64_tr_b16 a[192:193], v31 offset:0\n"
    "ds_read_b64_tr_b16 a[194:195], v31 offset:512\n"
    "ds_read_b64_tr_b16 a[196:197], v32 offset:0\n"
    "ds_read_b64_tr_b16 a[198:199], v32 offset:512\n"
    "ds_read_b64_tr_b16 a[200:201], v33 offset:0\n"
    "ds_read_b64_tr_b16 a[202:203], v33 offset:512\n"
    "s_waitcnt lgkmcnt(0)\n"
    "v_smfmac_f32_16x16x64_bf16 v[240:243], v[248:251], a[176:183], v252\n"
    "s_waitcnt vmcnt(11)\n"
    "s_barrier\n"
    "ds_read_b128 a[208:211], v26 offset:0\n"
    "v_smfmac_f32_16x16x64_bf16 v[244:247], v[248:251], a[184:191], v252\n"
    "ds_read_b128 a[212:215], v26 offset:2048\n"
    "s_add_u32 s55, s55, 1\n"
    "s_and_b32 s55, s55, 7\n"
    "s_lshl_b32 s57, s55, 7\n"
    "v_mfma_f32_16x16x32_bf16 v[128:131], a[176:179], a[160:163], 0\n"
    "ds_read_b128 a[216:219], v26 offset:4096\n"
    "v_mfma_f32_16x16x32_bf16 v[132:135], a[176:179], a[164:167], 0\n"
    "s_add_u32 m0, s59, 90112\n"
    "ds_read_b128 a[220:223], v26 offset:6144\n"
    "v_mfma_f32_16x16x32_bf16 v[136:139], a[176:179], a[168:171], 0\n"
    "ds_read_b64_tr_b16 a[224:225], v27 offset:12288\n"
    "buffer_load_dwordx4 v17, s[4:7], s57 offen offset:0 lds\n"
    "v_mfma_f32_16x16x32_bf16 v[140:143], a[176:179], a[172:175], 0\n"
    "ds_read_b64_tr_b16 a[226:227], v27 offset:12800\n"
    "v_mfma_f32_16x16x32_bf16 v[144:147], a[180:183], a[160:163], 0\n"
    "buffer_load_dwordx4 v18, s[4:7], s57 offen offset:1024 lds\n"
    "ds_read_b64_tr_b16 a[228:229], v28 offset:12288\n"
    "v_mfma_f32_16x16x32_bf16 v[148:151], a[180:183], a[164:167], 0\n"
    "ds_read_b64_tr_b16 a[230:231], v28 offset:12800\n"
    "buffer_load_dwordx4 v19, s[4:7], s57 offen offset:2048 lds\n"
    "v_mfma_f32_16x16x32_bf16 v[152:155], a[180:183], a[168:171], 0\n"
    "ds_read_b64_tr_b16 a[232:233], v29 offset:12288\n"
    "v_mfma_f32_16x16x32_bf16 v[156:159], a[180:183], a[172:175], 0\n"
    "s_add_u32 m0, s59, 90112\n"
    "s_mul_i32 s58, s55, s28\n"
    "ds_read_b64_tr_b16 a[234:235], v29 offset:12800\n"
    "v_mfma_f32_16x16x32_bf16 v[160:163], a[184:187], a[160:163], 0\n"
    "ds_read_b64_tr_b16 a[236:237], v30 offset:2048\n"
    "buffer_load_dwordx4 v20, s[4:7], s57 offen offset:3072 lds\n"
    "v_mfma_f32_16x16x32_bf16 v[164:167], a[184:187], a[164:167], 0\n"
    "ds_read_b64_tr_b16 a[238:239], v30 offset:2304\n"
    "v_mfma_f32_16x16x32_bf16 v[168:171], a[184:187], a[168:171], 0\n"
    "s_add_u32 m0, s61, 90112\n"
    "ds_read_b64_tr_b16 a[240:241], v31 offset:12288\n"
    "v_mfma_f32_16x16x32_bf16 v[172:175], a[184:187], a[172:175], 0\n"
    "ds_read_b64_tr_b16 a[242:243], v31 offset:12800\n"
    "buffer_load_dwordx4 v24, s[36:39], s58 offen offset:0 lds\n"
    "v_mfma_f32_16x16x32_bf16 v[176:179], a[188:191], a[160:163], 0\n"
    "ds_read_b64_tr_b16 a[244:245], v32 offset:12288\n"
    "v_mfma_f32_16x16x32_bf16 v[180:183], a[188:191], a[164:167], 0\n"
    "s_add_u32 s54, s54, 1\n"
    "s_and_b32 s54, s54, 15\n"
    "s_mul_i32 s56, s54, s29\n"
    "ds_read_b64_tr_b16 a[246:247], v32 offset:12800\n"
    "v_mfma_f32_16x16x32_bf16 v[184:187], a[188:191], a[168:171], 0\n"
    "ds_read_b64_tr_b16 a[248:249], v33 offset:12288\n"
    "s_add_u32 m0, s60, 0\n"
    "v_mfma_f32_16x16x32_bf16 v[188:191], a[188:191], a[172:175], 0\n"
    "ds_read_b64_tr_b16 a[250:251], v33 offset:12800\n"
    "v_mfma_f32_16x16x32_bf16 v[192:195], a[192:195], a[160:163], 0\n"
    "buffer_load_dwordx4 v21, s[8:11], s56 offen offset:0 lds\n"
    "ds_read_b32 v110, v39\n"
    "v_mfma_f32_16x16x32_bf16 v[196:199], a[192:195], a[164:167], 0\n"
    "v_mfma_f32_16x16x32_bf16 v[200:203], a[192:195], a[168:171], 0\n"
    "buffer_load_dwordx4 v22, s[8:11], s56 offen offset:1024 lds\n"
    "v_mfma_f32_16x16x32_bf16 v[204:207], a[192:195], a[172:175], 0\n"
    "v_mfma_f32_16x16x32_bf16 v[208:211], a[196:199], a[160:163], 0\n"
    "v_mfma_f32_16x16x32_bf16 v[212:215], a[196:199], a[164:167], 0\n"
    "buffer_load_dwordx4 v23, s[8:11], s56 offen offset:2048 lds\n"
    "v_mfma_f32_16x16x32_bf16 v[216:219], a[196:199], a[168:171], 0\n"
    "v_mfma_f32_16x16x32_bf16 v[220:223], a[196:199], a[172:175], 0\n"
    "v_mfma_f32_16x16x32_bf16 v[224:227], a[200:203], a[160:163], 0\n"
    "v_mfma_f32_16x16x32_bf16 v[228:231], a[200:203], a[164:167], 0\n"
    "v_mfma_f32_16x16x32_bf16 v[232:235], a[200:203], a[168:171], 0\n"
    "v_mfma_f32_16x16x32_bf16 v[236:239], a[200:203], a[172:175], 0\n"
    "s_waitcnt lgkmcnt(0)\n"
    "v_smfmac_f32_16x16x64_bf16 v[240:243], v[248:251], a[224:231], v252\n"
    "s_waitcnt vmcnt(11)\n"
    "s_barrier\n"
    "ds_read_b128 a[160:163], v25 offset:20480\n"
    "v_smfmac_f32_16x16x64_bf16 v[244:247], v[248:251], a[232:239], v252\n"
    "ds_read_b128 a[164:167], v25 offset:22528\n"
    "s_add_u32 s54, s54, 1\n"
    "s_and_b32 s54, s54, 15\n"
    "s_mul_i32 s56, s54, s29\n"
    "v_mfma_f32_16x16x32_bf16 v[128:131], a[224:227], a[208:211], v[128:131]\n"
    "ds_read_b128 a[168:171], v25 offset:24576\n"
    "v_mfma_f32_16x16x32_bf16 v[132:135], a[224:227], a[212:215], v[132:135]\n"
    "ds_read_b128 a[172:175], v25 offset:26624\n"
    "s_add_u32 m0, s60, 12288\n"
    "v_mfma_f32_16x16x32_bf16 v[136:139], a[224:227], a[216:219], v[136:139]\n"
    "ds_read_b64_tr_b16 a[176:177], v27 offset:24576\n"
    "v_mfma_f32_16x16x32_bf16 v[140:143], a[224:227], a[220:223], v[140:143]\n"
    "ds_read_b64_tr_b16 a[178:179], v27 offset:25088\n"
    "buffer_load_dwordx4 v21, s[8:11], s56 offen offset:0 lds\n"
    "v_mfma_f32_16x16x32_bf16 v[144:147], a[228:231], a[208:211], v[144:147]\n"
    "ds_read_b64_tr_b16 a[180:181], v28 offset:24576\n"
    "v_mfma_f32_16x16x32_bf16 v[148:151], a[228:231], a[212:215], v[148:151]\n"
    "ds_read_b64_tr_b16 a[182:183], v28 offset:25088\n"
    "buffer_load_dwordx4 v22, s[8:11], s56 offen offset:1024 lds\n"
    "v_mfma_f32_16x16x32_bf16 v[152:155], a[228:231], a[216:219], v[152:155]\n"
    "v_mov_b32 v111, s66\n"
    "ds_read_b64_tr_b16 a[184:185], v29 offset:24576\n"
    "v_mfma_f32_16x16x32_bf16 v[156:159], a[228:231], a[220:223], v[156:159]\n"
    "ds_read_b64_tr_b16 a[186:187], v29 offset:25088\n"
    "buffer_load_dwordx4 v23, s[8:11], s56 offen offset:2048 lds\n"
    "v_mfma_f32_16x16x32_bf16 v[160:163], a[232:235], a[208:211], v[160:163]\n"
    "ds_read_b64_tr_b16 a[188:189], v30 offset:20480\n"
    "v_mfma_f32_16x16x32_bf16 v[164:167], a[232:235], a[212:215], v[164:167]\n"
    "ds_read_b64_tr_b16 a[190:191], v30 offset:20736\n"
    "v_mfma_f32_16x16x32_bf16 v[168:171], a[232:235], a[216:219], v[168:171]\n"
    "s_add_u32 s55, s55, 1\n"
    "s_and_b32 s55, s55, 7\n"
    "s_lshl_b32 s57, s55, 7\n"
    "ds_read_b64_tr_b16 a[192:193], v31 offset:24576\n"
    "v_mfma_f32_16x16x32_bf16 v[172:175], a[232:235], a[220:223], v[172:175]\n"
    "ds_read_b64_tr_b16 a[194:195], v31 offset:25088\n"
    "v_mfma_f32_16x16x32_bf16 v[176:179], a[236:239], a[208:211], v[176:179]\n"
    "s_add_u32 m0, s59, 49152\n"
    "ds_read_b64_tr_b16 a[196:197], v32 offset:24576\n"
    "v_mfma_f32_16x16x32_bf16 v[180:183], a[236:239], a[212:215], v[180:183]\n"
    "ds_read_b64_tr_b16 a[198:199], v32 offset:25088\n"
    "buffer_store_dwordx2 v[110:111], v127, s[16:19], 0 offen sc1\n"
    "v_mfma_f32_16x16x32_bf16 v[184:187], a[236:239], a[216:219], v[184:187]\n"
    "buffer_load_dwordx4 v17, s[4:7], s57 offen offset:0 lds\n"
    "ds_read_b64_tr_b16 a[200:201], v33 offset:24576\n"
    "v_mfma_f32_16x16x32_bf16 v[188:191], a[236:239], a[220:223], v[188:191]\n"
    "ds_read_b64_tr_b16 a[202:203], v33 offset:25088\n"
    "v_mfma_f32_16x16x32_bf16 v[192:195], a[240:243], a[208:211], v[192:195]\n"
    "buffer_load_dwordx4 v18, s[4:7], s57 offen offset:1024 lds\n"
    "v_mfma_f32_16x16x32_bf16 v[196:199], a[240:243], a[212:215], v[196:199]\n"
    "v_mfma_f32_16x16x32_bf16 v[200:203], a[240:243], a[216:219], v[200:203]\n"
    "v_mfma_f32_16x16x32_bf16 v[204:207], a[240:243], a[220:223], v[204:207]\n"
    "v_mfma_f32_16x16x32_bf16 v[208:211], a[244:247], a[208:211], v[208:211]\n"
    "buffer_load_dwordx4 v19, s[4:7], s57 offen offset:2048 lds\n"
    "v_mfma_f32_16x16x32_bf16 v[212:215], a[244:247], a[212:215], v[212:215]\n"
    "v_mfma_f32_16x16x32_bf16 v[216:219], a[244:247], a[216:219], v[216:219]\n"
    "v_mfma_f32_16x16x32_bf16 v[220:223], a[244:247], a[220:223], v[220:223]\n"
    "v_mfma_f32_16x16x32_bf16 v[224:227], a[248:251], a[208:211], v[224:227]\n"
    "v_mfma_f32_16x16x32_bf16 v[228:231], a[248:251], a[212:215], v[228:231]\n"
    "v_mfma_f32_16x16x32_bf16 v[232:235], a[248:251], a[216:219], v[232:235]\n"
    "v_mfma_f32_16x16x32_bf16 v[236:239], a[248:251], a[220:223], v[236:239]\n"
    "s_waitcnt lgkmcnt(0)\n"
    "v_smfmac_f32_16x16x64_bf16 v[240:243], v[248:251], a[176:183], v252\n"
    "s_waitcnt vmcnt(15)\n"
    "s_barrier\n"
    "ds_read_b128 a[208:211], v26 offset:20480\n"
    "v_smfmac_f32_16x16x64_bf16 v[244:247], v[248:251], a[184:191], v252\n"
    "ds_read_b128 a[212:215], v26 offset:22528\n"
    "s_add_u32 m0, s59, 49152\n"
    "s_mul_i32 s58, s55, s28\n"
    "v_mfma_f32_16x16x32_bf16 v[128:131], a[176:179], a[160:163], v[128:131]\n"
    "ds_read_b128 a[216:219], v26 offset:24576\n"
    "v_mfma_f32_16x16x32_bf16 v[132:135], a[176:179], a[164:167], v[132:135]\n"
    "ds_read_b128 a[220:223], v26 offset:26624\n"
    "buffer_load_dwordx4 v20, s[4:7], s57 offen offset:3072 lds\n"
    "v_mfma_f32_16x16x32_bf16 v[136:139], a[176:179], a[168:171], v[136:139]\n"
    "ds_read_b64_tr_b16 a[224:225], v27 offset:36864\n"
    "v_mfma_f32_16x16x32_bf16 v[140:143], a[176:179], a[172:175], v[140:143]\n"
    "ds_read_b64_tr_b16 a[226:227], v27 offset:37376\n"
    "v_mfma_f32_16x16x32_bf16 v[144:147], a[180:183], a[160:163], v[144:147]\n"
    "s_add_u32 m0, s61, 49152\n"
    "ds_read_b64_tr_b16 a[228:229], v28 offset:36864\n"
    "v_mfma_f32_16x16x32_bf16 v[148:151], a[180:183], a[164:167], v[148:151]\n"
    "ds_read_b64_tr_b16 a[230:231], v28 offset:37376\n"
    "v_mfma_f32_16x16x32_bf16 v[152:155], a[180:183], a[168:171], v[152:155]\n"
    "ds_read_b64_tr_b16 a[232:233], v29 offset:36864\n"
    "buffer_load_dwordx4 v24, s[36:39], s58 offen offset:0 lds\n"
    "v_mfma_f32_16x16x32_bf16 v[156:159], a[180:183], a[172:175], v[156:159]\n"
    "ds_read_b64_tr_b16 a[234:235], v29 offset:37376\n"
    "v_mfma_f32_16x16x32_bf16 v[160:163], a[184:187], a[160:163], v[160:163]\n"
    "ds_read_b64_tr_b16 a[236:237], v30 offset:22528\n"
    "s_add_u32 s54, s54, 1\n"
    "s_and_b32 s54, s54, 15\n"
    "s_mul_i32 s56, s54, s29\n"
    "v_mfma_f32_16x16x32_bf16 v[164:167], a[184:187], a[164:167], v[164:167]\n"
    "ds_read_b64_tr_b16 a[238:239], v30 offset:22784\n"
    "v_mfma_f32_16x16x32_bf16 v[168:171], a[184:187], a[168:171], v[168:171]\n"
    "ds_read_b64_tr_b16 a[240:241], v31 offset:36864\n"
    "v_mfma_f32_16x16x32_bf16 v[172:175], a[184:187], a[172:175], v[172:175]\n"
    "s_add_u32 m0, s60, 24576\n"
    "ds_read_b64_tr_b16 a[242:243], v31 offset:37376\n"
    "v_mfma_f32_16x16x32_bf16 v[176:179], a[188:191], a[160:163], v[176:179]\n"
    "ds_read_b64_tr_b16 a[244:245], v32 offset:36864\n"
    "v_mfma_f32_16x16x32_bf16 v[180:183], a[188:191], a[164:167], v[180:183]\n"
    "ds_read_b64_tr_b16 a[246:247], v32 offset:37376\n"
    "buffer_load_dwordx4 v21, s[8:11], s56 offen offset:0 lds\n"
    "v_mfma_f32_16x16x32_bf16 v[184:187], a[188:191], a[168:171], v[184:187]\n"
    "ds_read_b64_tr_b16 a[248:249], v33 offset:36864\n"
    "v_mfma_f32_16x16x32_bf16 v[188:191], a[188:191], a[172:175], v[188:191]\n"
    "ds_read_b64_tr_b16 a[250:251], v33 offset:37376\n"
    "buffer_load_dwordx4 v22, s[8:11], s56 offen offset:1024 lds\n"
    "v_mfma_f32_16x16x32_bf16 v[192:195], a[192:195], a[160:163], v[192:195]\n"
    "v_mfma_f32_16x16x32_bf16 v[196:199], a[192:195], a[164:167], v[196:199]\n"
    "v_mfma_f32_16x16x32_bf16 v[200:203], a[192:195], a[168:171], v[200:203]\n"
    "v_mfma_f32_16x16x32_bf16 v[204:207], a[192:195], a[172:175], v[204:207]\n"
    "v_mfma_f32_16x16x32_bf16 v[208:211], a[196:199], a[160:163], v[208:211]\n"
    "buffer_load_dwordx4 v23, s[8:11], s56 offen offset:2048 lds\n"
    "v_mfma_f32_16x16x32_bf16 v[212:215], a[196:199], a[164:167], v[212:215]\n"
    "v_mfma_f32_16x16x32_bf16 v[216:219], a[196:199], a[168:171], v[216:219]\n"
    "v_mfma_f32_16x16x32_bf16 v[220:223], a[196:199], a[172:175], v[220:223]\n"
    "v_mfma_f32_16x16x32_bf16 v[224:227], a[200:203], a[160:163], v[224:227]\n"
    "v_mfma_f32_16x16x32_bf16 v[228:231], a[200:203], a[164:167], v[228:231]\n"
    "v_mfma_f32_16x16x32_bf16 v[232:235], a[200:203], a[168:171], v[232:235]\n"
    "v_mfma_f32_16x16x32_bf16 v[236:239], a[200:203], a[172:175], v[236:239]\n"
    "s_waitcnt lgkmcnt(0)\n"
    "v_smfmac_f32_16x16x64_bf16 v[240:243], v[248:251], a[224:231], v252\n"
    "s_waitcnt vmcnt(12)\n"
    "s_barrier\n"
    "ds_read_b128 a[160:163], v25 offset:40960\n"
    "v_smfmac_f32_16x16x64_bf16 v[244:247], v[248:251], a[232:239], v252\n"
    "ds_read_b128 a[164:167], v25 offset:43008\n"
    "s_add_u32 s54, s54, 1\n"
    "s_and_b32 s54, s54, 15\n"
    "s_mul_i32 s56, s54, s29\n"
    "v_mfma_f32_16x16x32_bf16 v[128:131], a[224:227], a[208:211], v[128:131]\n"
    "ds_read_b128 a[168:171], v25 offset:45056\n"
    "v_mfma_f32_16x16x32_bf16 v[132:135], a[224:227], a[212:215], v[132:135]\n"
    "ds_read_b128 a[172:175], v25 offset:47104\n"
    "s_add_u32 m0, s60, 36864\n"
    "v_mfma_f32_16x16x32_bf16 v[136:139], a[224:227], a[216:219], v[136:139]\n"
    "ds_read_b64_tr_b16 a[176:177], v27 offset:0\n"
    "v_mfma_f32_16x16x32_bf16 v[140:143], a[224:227], a[220:223], v[140:143]\n"
    "ds_read_b64_tr_b16 a[178:179], v27 offset:512\n"
    "buffer_load_dwordx4 v21, s[8:11], s56 offen offset:0 lds\n"
    "v_mfma_f32_16x16x32_bf16 v[144:147], a[228:231], a[208:211], v[144:147]\n"
    "ds_read_b64_tr_b16 a[180:181], v28 offset:0\n"
    "v_mfma_f32_16x16x32_bf16 v[148:151], a[228:231], a[212:215], v[148:151]\n"
    "ds_read_b64_tr_b16 a[182:183], v28 offset:512\n"
    "buffer_load_dwordx4 v22, s[8:11], s56 offen offset:1024 lds\n"
    "v_mfma_f32_16x16x32_bf16 v[152:155], a[228:231], a[216:219], v[152:155]\n"
    "ds_read_b64_tr_b16 a[184:185], v29 offset:0\n"
    "v_mfma_f32_16x16x32_bf16 v[156:159], a[228:231], a[220:223], v[156:159]\n"
    "ds_read_b64_tr_b16 a[186:187], v29 offset:512\n"
    "buffer_load_dwordx4 v23, s[8:11], s56 offen offset:2048 lds\n"
    "v_mfma_f32_16x16x32_bf16 v[160:163], a[232:235], a[208:211], v[160:163]\n"
    "ds_read_b64_tr_b16 a[188:189], v30 offset:40960\n"
    "v_mfma_f32_16x16x32_bf16 v[164:167], a[232:235], a[212:215], v[164:167]\n"
    "ds_read_b64_tr_b16 a[190:191], v30 offset:41216\n"
    "v_mfma_f32_16x16x32_bf16 v[168:171], a[232:235], a[216:219], v[168:171]\n"
    "s_add_u32 s55, s55, 1\n"
    "s_and_b32 s55, s55, 7\n"
    "s_lshl_b32 s57, s55, 7\n"
    "ds_read_b64_tr_b16 a[192:193], v31 offset:0\n"
    "v_mfma_f32_16x16x32_bf16 v[172:175], a[232:235], a[220:223], v[172:175]\n"
    "ds_read_b64_tr_b16 a[194:195], v31 offset:512\n"
    "v_mfma_f32_16x16x32_bf16 v[176:179], a[236:239], a[208:211], v[176:179]\n"
    "s_add_u32 m0, s59, 69632\n"
    "ds_read_b64_tr_b16 a[196:197], v32 offset:0\n"
    "v_mfma_f32_16x16x32_bf16 v[180:183], a[236:239], a[212:215], v[180:183]\n"
    "ds_read_b64_tr_b16 a[198:199], v32 offset:512\n"
    "v_mfma_f32_16x16x32_bf16 v[184:187], a[236:239], a[216:219], v[184:187]\n"
    "buffer_load_dwordx4 v17, s[4:7], s57 offen offset:0 lds\n"
    "ds_read_b64_tr_b16 a[200:201], v33 offset:0\n"
    "v_mfma_f32_16x16x32_bf16 v[188:191], a[236:239], a[220:223], v[188:191]\n"
    "ds_read_b64_tr_b16 a[202:203], v33 offset:512\n"
    "v_mfma_f32_16x16x32_bf16 v[192:195], a[240:243], a[208:211], v[192:195]\n"
    "buffer_load_dwordx4 v18, s[4:7], s57 offen offset:1024 lds\n"
    "v_mfma_f32_16x16x32_bf16 v[196:199], a[240:243], a[212:215], v[196:199]\n"
    "v_mfma_f32_16x16x32_bf16 v[200:203], a[240:243], a[216:219], v[200:203]\n"
    "v_mfma_f32_16x16x32_bf16 v[204:207], a[240:243], a[220:223], v[204:207]\n"
    "v_mfma_f32_16x16x32_bf16 v[208:211], a[244:247], a[208:211], v[208:211]\n"
    "buffer_load_dwordx4 v19, s[4:7], s57 offen offset:2048 lds\n"
    "v_mfma_f32_16x16x32_bf16 v[212:215], a[244:247], a[212:215], v[212:215]\n"
    "v_mfma_f32_16x16x32_bf16 v[216:219], a[244:247], a[216:219], v[216:219]\n"
    "v_mfma_f32_16x16x32_bf16 v[220:223], a[244:247], a[220:223], v[220:223]\n"
    "v_mfma_f32_16x16x32_bf16 v[224:227], a[248:251], a[208:211], v[224:227]\n"
    "v_mfma_f32_16x16x32_bf16 v[228:231], a[248:251], a[212:215], v[228:231]\n"
    "v_mfma_f32_16x16x32_bf16 v[232:235], a[248:251], a[216:219], v[232:235]\n"
    "v_mfma_f32_16x16x32_bf16 v[236:239], a[248:251], a[220:223], v[236:239]\n"
    "s_waitcnt lgkmcnt(0)\n"
    "v_smfmac_f32_16x16x64_bf16 v[240:243], v[248:251], a[176:183], v252\n"
    "s_waitcnt vmcnt(15)\n"
    "s_barrier\n"
    "ds_read_b128 a[208:211], v26 offset:40960\n"
    "v_smfmac_f32_16x16x64_bf16 v[244:247], v[248:251], a[184:191], v252\n"
    "ds_read_b128 a[212:215], v26 offset:43008\n"
    "s_add_u32 m0, s59, 69632\n"
    "s_mul_i32 s58, s55, s28\n"
    "v_mfma_f32_16x16x32_bf16 v[128:131], a[176:179], a[160:163], v[128:131]\n"
    "ds_read_b128 a[216:219], v26 offset:45056\n"
    "v_mfma_f32_16x16x32_bf16 v[132:135], a[176:179], a[164:167], v[132:135]\n"
    "ds_read_b128 a[220:223], v26 offset:47104\n"
    "buffer_load_dwordx4 v20, s[4:7], s57 offen offset:3072 lds\n"
    "v_mfma_f32_16x16x32_bf16 v[136:139], a[176:179], a[168:171], v[136:139]\n"
    "ds_read_b64_tr_b16 a[224:225], v27 offset:12288\n"
    "v_mfma_f32_16x16x32_bf16 v[140:143], a[176:179], a[172:175], v[140:143]\n"
    "ds_read_b64_tr_b16 a[226:227], v27 offset:12800\n"
    "v_mfma_f32_16x16x32_bf16 v[144:147], a[180:183], a[160:163], v[144:147]\n"
    "s_add_u32 m0, s61, 69632\n"
    "ds_read_b64_tr_b16 a[228:229], v28 offset:12288\n"
    "v_mfma_f32_16x16x32_bf16 v[148:151], a[180:183], a[164:167], v[148:151]\n"
    "ds_read_b64_tr_b16 a[230:231], v28 offset:12800\n"
    "v_mfma_f32_16x16x32_bf16 v[152:155], a[180:183], a[168:171], v[152:155]\n"
    "ds_read_b64_tr_b16 a[232:233], v29 offset:12288\n"
    "buffer_load_dwordx4 v24, s[36:39], s58 offen offset:0 lds\n"
    "v_mfma_f32_16x16x32_bf16 v[156:159], a[180:183], a[172:175], v[156:159]\n"
    "ds_read_b64_tr_b16 a[234:235], v29 offset:12800\n"
    "v_mfma_f32_16x16x32_bf16 v[160:163], a[184:187], a[160:163], v[160:163]\n"
    "ds_read_b64_tr_b16 a[236:237], v30 offset:43008\n"
    "s_add_u32 s54, s54, 1\n"
    "s_and_b32 s54, s54, 15\n"
    "s_mul_i32 s56, s54, s29\n"
    "v_mfma_f32_16x16x32_bf16 v[164:167], a[184:187], a[164:167], v[164:167]\n"
    "ds_read_b64_tr_b16 a[238:239], v30 offset:43264\n"
    "v_mfma_f32_16x16x32_bf16 v[168:171], a[184:187], a[168:171], v[168:171]\n"
    "ds_read_b64_tr_b16 a[240:241], v31 offset:12288\n"
    "v_mfma_f32_16x16x32_bf16 v[172:175], a[184:187], a[172:175], v[172:175]\n"
    "s_add_u32 m0, s60, 0\n"
    "ds_read_b64_tr_b16 a[242:243], v31 offset:12800\n"
    "v_mfma_f32_16x16x32_bf16 v[176:179], a[188:191], a[160:163], v[176:179]\n"
    "ds_read_b64_tr_b16 a[244:245], v32 offset:12288\n"
    "v_mfma_f32_16x16x32_bf16 v[180:183], a[188:191], a[164:167], v[180:183]\n"
    "ds_read_b64_tr_b16 a[246:247], v32 offset:12800\n"
    "buffer_load_dwordx4 v21, s[8:11], s56 offen offset:0 lds\n"
    "v_mfma_f32_16x16x32_bf16 v[184:187], a[188:191], a[168:171], v[184:187]\n"
    "ds_read_b64_tr_b16 a[248:249], v33 offset:12288\n"
    "v_mfma_f32_16x16x32_bf16 v[188:191], a[188:191], a[172:175], v[188:191]\n"
    "ds_read_b64_tr_b16 a[250:251], v33 offset:12800\n"
    "buffer_load_dwordx4 v22, s[8:11], s56 offen offset:1024 lds\n"
    "v_mfma_f32_16x16x32_bf16 v[192:195], a[192:195], a[160:163], v[192:195]\n"
    "v_mfma_f32_16x16x32_bf16 v[196:199], a[192:195], a[164:167], v[196:199]\n"
    "v_mfma_f32_16x16x32_bf16 v[200:203], a[192:195], a[168:171], v[200:203]\n"
    "v_mfma_f32_16x16x32_bf16 v[204:207], a[192:195], a[172:175], v[204:207]\n"
    "v_mfma_f32_16x16x32_bf16 v[208:211], a[196:199], a[160:163], v[208:211]\n"
    "buffer_load_dwordx4 v23, s[8:11], s56 offen offset:2048 lds\n"
    "v_mfma_f32_16x16x32_bf16 v[212:215], a[196:199], a[164:167], v[212:215]\n"
    "v_mfma_f32_16x16x32_bf16 v[216:219], a[196:199], a[168:171], v[216:219]\n"
    "v_mfma_f32_16x16x32_bf16 v[220:223], a[196:199], a[172:175], v[220:223]\n"
    "v_mfma_f32_16x16x32_bf16 v[224:227], a[200:203], a[160:163], v[224:227]\n"
    "v_mfma_f32_16x16x32_bf16 v[228:231], a[200:203], a[164:167], v[228:231]\n"
    "v_mfma_f32_16x16x32_bf16 v[232:235], a[200:203], a[168:171], v[232:235]\n"
    "v_mfma_f32_16x16x32_bf16 v[236:239], a[200:203], a[172:175], v[236:239]\n"
    "s_waitcnt lgkmcnt(0)\n"
    "v_smfmac_f32_16x16x64_bf16 v[240:243], v[248:251], a[224:231], v252\n"
    "s_waitcnt vmcnt(11)\n"
    "s_barrier\n"
    "ds_read_b128 a[160:163], v25 offset:0\n"
    "v_smfmac_f32_16x16x64_bf16 v[244:247], v[248:251], a[232:239], v252\n"
    "ds_read_b128 a[164:167], v25 offset:2048\n"
    "s_add_u32 s54, s54, 1\n"
    "s_and_b32 s54, s54, 15\n"
    "s_mul_i32 s56, s54, s29\n"
    "v_mfma_f32_16x16x32_bf16 v[128:131], a[224:227], a[208:211], v[128:131]\n"
    "ds_read_b128 a[168:171], v25 offset:4096\n"
    "v_mfma_f32_16x16x32_bf16 v[132:135], a[224:227], a[212:215], v[132:135]\n"
    "ds_read_b128 a[172:175], v25 offset:6144\n"
    "s_add_u32 m0, s60, 12288\n"
    "v_mfma_f32_16x16x32_bf16 v[136:139], a[224:227], a[216:219], v[136:139]\n"
    "ds_read_b64_tr_b16 a[176:177], v27 offset:24576\n"
    "v_mfma_f32_16x16x32_bf16 v[140:143], a[224:227], a[220:223], v[140:143]\n"
    "ds_read_b64_tr_b16 a[178:179], v27 offset:25088\n"
    "buffer_load_dwordx4 v21, s[8:11], s56 offen offset:0 lds\n"
    "v_mfma_f32_16x16x32_bf16 v[144:147], a[228:231], a[208:211], v[144:147]\n"
    "ds_read_b64_tr_b16 a[180:181], v28 offset:24576\n"
    "v_mfma_f32_16x16x32_bf16 v[148:151], a[228:231], a[212:215], v[148:151]\n"
    "ds_read_b64_tr_b16 a[182:183], v28 offset:25088\n"
    "buffer_load_dwordx4 v22, s[8:11], s56 offen offset:1024 lds\n"
    "v_mfma_f32_16x16x32_bf16 v[152:155], a[228:231], a[216:219], v[152:155]\n"
    "ds_read_b64_tr_b16 a[184:185], v29 offset:24576\n"
    "v_mfma_f32_16x16x32_bf16 v[156:159], a[228:231], a[220:223], v[156:159]\n"
    "ds_read_b64_tr_b16 a[186:187], v29 offset:25088\n"
    "buffer_load_dwordx4 v23, s[8:11], s56 offen offset:2048 lds\n"
    "v_mfma_f32_16x16x32_bf16 v[160:163], a[232:235], a[208:211], v[160:163]\n"
    "ds_read_b64_tr_b16 a[188:189], v30 offset:0\n"
    "v_mfma_f32_16x16x32_bf16 v[164:167], a[232:235], a[212:215], v[164:167]\n"
    "ds_read_b64_tr_b16 a[190:191], v30 offset:256\n"
    "v_mfma_f32_16x16x32_bf16 v[168:171], a[232:235], a[216:219], v[168:171]\n"
    "s_add_u32 s55, s55, 1\n"
    "s_and_b32 s55, s55, 7\n"
    "s_lshl_b32 s57, s55, 7\n"
    "ds_read_b64_tr_b16 a[192:193], v31 offset:24576\n"
    "v_mfma_f32_16x16x32_bf16 v[172:175], a[232:235], a[220:223], v[172:175]\n"
    "ds_read_b64_tr_b16 a[194:195], v31 offset:25088\n"
    "v_mfma_f32_16x16x32_bf16 v[176:179], a[236:239], a[208:211], v[176:179]\n"
    "s_add_u32 m0, s59, 90112\n"
    "ds_read_b64_tr_b16 a[196:197], v32 offset:24576\n"
    "v_mfma_f32_16x16x32_bf16 v[180:183], a[236:239], a[212:215], v[180:183]\n"
    "ds_read_b64_tr_b16 a[198:199], v32 offset:25088\n"
    "v_mfma_f32_16x16x32_bf16 v[184:187], a[236:239], a[216:219], v[184:187]\n"
    "buffer_load_dwordx4 v17, s[4:7], s57 offen offset:0 lds\n"
    "ds_read_b64_tr_b16 a[200:201], v33 offset:24576\n"
    "v_mfma_f32_16x16x32_bf16 v[188:191], a[236:239], a[220:223], v[188:191]\n"
    "ds_read_b64_tr_b16 a[202:203], v33 offset:25088\n"
    "v_mfma_f32_16x16x32_bf16 v[192:195], a[240:243], a[208:211], v[192:195]\n"
    "buffer_load_dwordx4 v18, s[4:7], s57 offen offset:1024 lds\n"
    "v_mfma_f32_16x16x32_bf16 v[196:199], a[240:243], a[212:215], v[196:199]\n"
    "v_mfma_f32_16x16x32_bf16 v[200:203], a[240:243], a[216:219], v[200:203]\n"
    "v_mfma_f32_16x16x32_bf16 v[204:207], a[240:243], a[220:223], v[204:207]\n"
    "v_mfma_f32_16x16x32_bf16 v[208:211], a[244:247], a[208:211], v[208:211]\n"
    "buffer_load_dwordx4 v19, s[4:7], s57 offen offset:2048 lds\n"
    "v_mfma_f32_16x16x32_bf16 v[212:215], a[244:247], a[212:215], v[212:215]\n"
    "v_mfma_f32_16x16x32_bf16 v[216:219], a[244:247], a[216:219], v[216:219]\n"
    "v_mfma_f32_16x16x32_bf16 v[220:223], a[244:247], a[220:223], v[220:223]\n"
    "v_mfma_f32_16x16x32_bf16 v[224:227], a[248:251], a[208:211], v[224:227]\n"
    "v_mfma_f32_16x16x32_bf16 v[228:231], a[248:251], a[212:215], v[228:231]\n"
    "v_mfma_f32_16x16x32_bf16 v[232:235], a[248:251], a[216:219], v[232:235]\n"
    "v_mfma_f32_16x16x32_bf16 v[236:239], a[248:251], a[220:223], v[236:239]\n"
    "s_waitcnt lgkmcnt(0)\n"
    "v_smfmac_f32_16x16x64_bf16 v[240:243], v[248:251], a[176:183], v252\n"
    "s_waitcnt vmcnt(14)\n"
    "s_barrier\n"
    "ds_read_b128 a[208:211], v26 offset:0\n"
    "v_smfmac_f32_16x16x64_bf16 v[244:247], v[248:251], a[184:191], v252\n"
    "ds_read_b128 a[212:215], v26 offset:2048\n"
    "s_add_u32 m0, s59, 90112\n"
    "s_mul_i32 s58, s55, s28\n"
    "v_mfma_f32_16x16x32_bf16 v[128:131], a[176:179], a[160:163], v[128:131]\n"
    "ds_read_b128 a[216:219], v26 offset:4096\n"
    "v_mfma_f32_16x16x32_bf16 v[132:135], a[176:179], a[164:167], v[132:135]\n"
    "ds_read_b128 a[220:223], v26 offset:6144\n"
    "buffer_load_dwordx4 v20, s[4:7], s57 offen offset:3072 lds\n"
    "v_mfma_f32_16x16x32_bf16 v[136:139], a[176:179], a[168:171], v[136:139]\n"
    "ds_read_b64_tr_b16 a[224:225], v27 offset:36864\n"
    "v_mfma_f32_16x16x32_bf16 v[140:143], a[176:179], a[172:175], v[140:143]\n"
    "ds_read_b64_tr_b16 a[226:227], v27 offset:37376\n"
    "v_mfma_f32_16x16x32_bf16 v[144:147], a[180:183], a[160:163], v[144:147]\n"
    "s_add_u32 m0, s61, 90112\n"
    "ds_read_b64_tr_b16 a[228:229], v28 offset:36864\n"
    "v_mfma_f32_16x16x32_bf16 v[148:151], a[180:183], a[164:167], v[148:151]\n"
    "ds_read_b64_tr_b16 a[230:231], v28 offset:37376\n"
    "v_mfma_f32_16x16x32_bf16 v[152:155], a[180:183], a[168:171], v[152:155]\n"
    "ds_read_b64_tr_b16 a[232:233], v29 offset:36864\n"
    "buffer_load_dwordx4 v24, s[36:39], s58 offen offset:0 lds\n"
    "v_mfma_f32_16x16x32_bf16 v[156:159], a[180:183], a[172:175], v[156:159]\n"
    "ds_read_b64_tr_b16 a[234:235], v29 offset:37376\n"
    "v_mfma_f32_16x16x32_bf16 v[160:163], a[184:187], a[160:163], v[160:163]\n"
    "ds_read_b64_tr_b16 a[236:237], v30 offset:2048\n"
    "s_add_u32 s54, s54, 1\n"
    "s_and_b32 s54, s54, 15\n"
    "s_mul_i32 s56, s54, s29\n"
    "v_mfma_f32_16x16x32_bf16 v[164:167], a[184:187], a[164:167], v[164:167]\n"
    "ds_read_b64_tr_b16 a[238:239], v30 offset:2304\n"
    "v_mfma_f32_16x16x32_bf16 v[168:171], a[184:187], a[168:171], v[168:171]\n"
    "ds_read_b64_tr_b16 a[240:241], v31 offset:36864\n"
    "v_mfma_f32_16x16x32_bf16 v[172:175], a[184:187], a[172:175], v[172:175]\n"
    "s_add_u32 m0, s60, 24576\n"
    "ds_read_b64_tr_b16 a[242:243], v31 offset:37376\n"
    "v_mfma_f32_16x16x32_bf16 v[176:179], a[188:191], a[160:163], v[176:179]\n"
    "ds_read_b64_tr_b16 a[244:245], v32 offset:36864\n"
    "v_mfma_f32_16x16x32_bf16 v[180:183], a[188:191], a[164:167], v[180:183]\n"
    "ds_read_b64_tr_b16 a[246:247], v32 offset:37376\n"
    "buffer_load_dwordx4 v21, s[8:11], s56 offen offset:0 lds\n"
    "v_mfma_f32_16x16x32_bf16 v[184:187], a[188:191], a[168:171], v[184:187]\n"
    "ds_read_b64_tr_b16 a[248:249], v33 offset:36864\n"
    "v_mfma_f32_16x16x32_bf16 v[188:191], a[188:191], a[172:175], v[188:191]\n"
    "ds_read_b64_tr_b16 a[250:251], v33 offset:37376\n"
    "buffer_load_dwordx4 v22, s[8:11], s56 offen offset:1024 lds\n"
    "v_mfma_f32_16x16x32_bf16 v[192:195], a[192:195], a[160:163], v[192:195]\n"
    "v_mfma_f32_16x16x32_bf16 v[196:199], a[192:195], a[164:167], v[196:199]\n"
    "v_mfma_f32_16x16x32_bf16 v[200:203], a[192:195], a[168:171], v[200:203]\n"
    "v_mfma_f32_16x16x32_bf16 v[204:207], a[192:195], a[172:175], v[204:207]\n"
    "v_mfma_f32_16x16x32_bf16 v[208:211], a[196:199], a[160:163], v[208:211]\n"
    "buffer_load_dwordx4 v23, s[8:11], s56 offen offset:2048 lds\n"
    "v_mfma_f32_16x16x32_bf16 v[212:215], a[196:199], a[164:167], v[212:215]\n"
    "v_mfma_f32_16x16x32_bf16 v[216:219], a[196:199], a[168:171], v[216:219]\n"
    "v_mfma_f32_16x16x32_bf16 v[220:223], a[196:199], a[172:175], v[220:223]\n"
    "v_mfma_f32_16x16x32_bf16 v[224:227], a[200:203], a[160:163], v[224:227]\n"
    "v_mfma_f32_16x16x32_bf16 v[228:231], a[200:203], a[164:167], v[228:231]\n"
    "v_mfma_f32_16x16x32_bf16 v[232:235], a[200:203], a[168:171], v[232:235]\n"
    "v_mfma_f32_16x16x32_bf16 v[236:239], a[200:203], a[172:175], v[236:239]\n"
    "s_waitcnt lgkmcnt(0)\n"
    "v_smfmac_f32_16x16x64_bf16 v[240:243], v[248:251], a[224:231], v252\n"
    "s_waitcnt vmcnt(11)\n"
    "s_barrier\n"
    "ds_read_b128 a[160:163], v25 offset:20480\n"
    "v_smfmac_f32_16x16x64_bf16 v[244:247], v[248:251], a[232:239], v252\n"
    "ds_read_b128 a[164:167], v25 offset:22528\n"
    "s_add_u32 s54, s54, 1\n"
    "s_and_b32 s54, s54, 15\n"
    "s_mul_i32 s56, s54, s29\n"
    "v_mfma_f32_16x16x32_bf16 v[128:131], a[224:227], a[208:211], v[128:131]\n"
    "ds_read_b128 a[168:171], v25 offset:24576\n"
    "v_mfma_f32_16x16x32_bf16 v[132:135], a[224:227], a[212:215], v[132:135]\n"
    "ds_read_b128 a[172:175], v25 offset:26624\n"
    "s_add_u32 m0, s60, 36864\n"
    "v_mfma_f32_16x16x32_bf16 v[136:139], a[224:227], a[216:219], v[136:139]\n"
    "ds_read_b64_tr_b16 a[176:177], v27 offset:0\n"
    "v_mfma_f32_16x16x32_bf16 v[140:143], a[224:227], a[220:223], v[140:143]\n"
    "ds_read_b64_tr_b16 a[178:179], v27 offset:512\n"
    "buffer_load_dwordx4 v21, s[8:11], s56 offen offset:0 lds\n"
    "v_mfma_f32_16x16x32_bf16 v[144:147], a[228:231], a[208:211], v[144:147]\n"
    "ds_read_b64_tr_b16 a[180:181], v28 offset:0\n"
    "v_mfma_f32_16x16x32_bf16 v[148:151], a[228:231], a[212:215], v[148:151]\n"
    "ds_read_b64_tr_b16 a[182:183], v28 offset:512\n"
    "buffer_load_dwordx4 v22, s[8:11], s56 offen offset:1024 lds\n"
    "v_mfma_f32_16x16x32_bf16 v[152:155], a[228:231], a[216:219], v[152:155]\n"
    "ds_read_b64_tr_b16 a[184:185], v29 offset:0\n"
    "v_mfma_f32_16x16x32_bf16 v[156:159], a[228:231], a[220:223], v[156:159]\n"
    "ds_read_b64_tr_b16 a[186:187], v29 offset:512\n"
    "buffer_load_dwordx4 v23, s[8:11], s56 offen offset:2048 lds\n"
    "v_mfma_f32_16x16x32_bf16 v[160:163], a[232:235], a[208:211], v[160:163]\n"
    "ds_read_b64_tr_b16 a[188:189], v30 offset:20480\n"
    "v_mfma_f32_16x16x32_bf16 v[164:167], a[232:235], a[212:215], v[164:167]\n"
    "ds_read_b64_tr_b16 a[190:191], v30 offset:20736\n"
    "v_mfma_f32_16x16x32_bf16 v[168:171], a[232:235], a[216:219], v[168:171]\n"
    "s_add_u32 s55, s55, 1\n"
    "s_and_b32 s55, s55, 7\n"
    "s_lshl_b32 s57, s55, 7\n"
    "ds_read_b64_tr_b16 a[192:193], v31 offset:0\n"
    "v_mfma_f32_16x16x32_bf16 v[172:175], a[232:235], a[220:223], v[172:175]\n"
    "ds_read_b64_tr_b16 a[194:195], v31 offset:512\n"
    "v_mfma_f32_16x16x32_bf16 v[176:179], a[236:239], a[208:211], v[176:179]\n"
    "s_add_u32 m0, s59, 49152\n"
    "ds_read_b64_tr_b16 a[196:197], v32 offset:0\n"
    "v_mfma_f32_16x16x32_bf16 v[180:183], a[236:239], a[212:215], v[180:183]\n"
    "ds_read_b64_tr_b16 a[198:199], v32 offset:512\n"
    "v_mfma_f32_16x16x32_bf16 v[184:187], a[236:239], a[216:219], v[184:187]\n"
    "buffer_load_dwordx4 v17, s[4:7], s57 offen offset:0 lds\n"
    "ds_read_b64_tr_b16 a[200:201], v33 offset:0\n"
    "v_mfma_f32_16x16x32_bf16 v[188:191], a[236:239], a[220:223], v[188:191]\n"
    "ds_read_b64_tr_b16 a[202:203], v33 offset:512\n"
    "v_mfma_f32_16x16x32_bf16 v[192:195], a[240:243], a[208:211], v[192:195]\n"
    "buffer_load_dwordx4 v18, s[4:7], s57 offen offset:1024 lds\n"
    "v_mfma_f32_16x16x32_bf16 v[196:199], a[240:243], a[212:215], v[196:199]\n"
    "v_mfma_f32_16x16x32_bf16 v[200:203], a[240:243], a[216:219], v[200:203]\n"
    "v_mfma_f32_16x16x32_bf16 v[204:207], a[240:243], a[220:223], v[204:207]\n"
    "v_mfma_f32_16x16x32_bf16 v[208:211], a[244:247], a[208:211], v[208:211]\n"
    "buffer_load_dwordx4 v19, s[4:7], s57 offen offset:2048 lds\n"
    "v_mfma_f32_16x16x32_bf16 v[212:215], a[244:247], a[212:215], v[212:215]\n"
    "v_mfma_f32_16x16x32_bf16 v[216:219], a[244:247], a[216:219], v[216:219]\n"
    "v_mfma_f32_16x16x32_bf16 v[220:223], a[244:247], a[220:223], v[220:223]\n"
    "v_mfma_f32_16x16x32_bf16 v[224:227], a[248:251], a[208:211], v[224:227]\n"
    "v_mfma_f32_16x16x32_bf16 v[228:231], a[248:251], a[212:215], v[228:231]\n"
    "v_mfma_f32_16x16x32_bf16 v[232:235], a[248:251], a[216:219], v[232:235]\n"
    "v_mfma_f32_16x16x32_bf16 v[236:239], a[248:251], a[220:223], v[236:239]\n"
    "s_waitcnt lgkmcnt(0)\n"
    "v_smfmac_f32_16x16x64_bf16 v[240:243], v[248:251], a[176:183], v252\n"
    "s_waitcnt vmcnt(14)\n"
    "s_barrier\n"
    "ds_read_b128 a[208:211], v26 offset:20480\n"
    "v_smfmac_f32_16x16x64_bf16 v[244:247], v[248:251], a[184:191], v252\n"
    "ds_read_b128 a[212:215], v26 offset:22528\n"
    "s_add_u32 m0, s59, 49152\n"
    "s_mul_i32 s58, s55, s28\n"
    "v_mfma_f32_16x16x32_bf16 v[128:131], a[176:179], a[160:163], v[128:131]\n"
    "ds_read_b128 a[216:219], v26 offset:24576\n"
    "v_mfma_f32_16x16x32_bf16 v[132:135], a[176:179], a[164:167], v[132:135]\n"
    "ds_read_b128 a[220:223], v26 offset:26624\n"
    "buffer_load_dwordx4 v20, s[4:7], s57 offen offset:3072 lds\n"
    "v_mfma_f32_16x16x32_bf16 v[136:139], a[176:179], a[168:171], v[136:139]\n"
    "ds_read_b64_tr_b16 a[224:225], v27 offset:12288\n"
    "v_mfma_f32_16x16x32_bf16 v[140:143], a[176:179], a[172:175], v[140:143]\n"
    "ds_read_b64_tr_b16 a[226:227], v27 offset:12800\n"
    "v_mfma_f32_16x16x32_bf16 v[144:147], a[180:183], a[160:163], v[144:147]\n"
    "s_add_u32 m0, s61, 49152\n"
    "ds_read_b64_tr_b16 a[228:229], v28 offset:12288\n"
    "v_mfma_f32_16x16x32_bf16 v[148:151], a[180:183], a[164:167], v[148:151]\n"
    "ds_read_b64_tr_b16 a[230:231], v28 offset:12800\n"
    "v_mfma_f32_16x16x32_bf16 v[152:155], a[180:183], a[168:171], v[152:155]\n"
    "ds_read_b64_tr_b16 a[232:233], v29 offset:12288\n"
    "buffer_load_dwordx4 v24, s[36:39], s58 offen offset:0 lds\n"
    "v_mfma_f32_16x16x32_bf16 v[156:159], a[180:183], a[172:175], v[156:159]\n"
    "ds_read_b64_tr_b16 a[234:235], v29 offset:12800\n"
    "v_mfma_f32_16x16x32_bf16 v[160:163], a[184:187], a[160:163], v[160:163]\n"
    "ds_read_b64_tr_b16 a[236:237], v30 offset:22528\n"
    "s_add_u32 s54, s54, 1\n"
    "s_and_b32 s54, s54, 15\n"
    "s_mul_i32 s56, s54, s29\n"
    "v_mfma_f32_16x16x32_bf16 v[164:167], a[184:187], a[164:167], v[164:167]\n"
    "ds_read_b64_tr_b16 a[238:239], v30 offset:22784\n"
    "v_mfma_f32_16x16x32_bf16 v[168:171], a[184:187], a[168:171], v[168:171]\n"
    "ds_read_b64_tr_b16 a[240:241], v31 offset:12288\n"
    "v_mfma_f32_16x16x32_bf16 v[172:175], a[184:187], a[172:175], v[172:175]\n"
    "s_add_u32 m0, s60, 0\n"
    "ds_read_b64_tr_b16 a[242:243], v31 offset:12800\n"
    "v_mfma_f32_16x16x32_bf16 v[176:179], a[188:191], a[160:163], v[176:179]\n"
    "ds_read_b64_tr_b16 a[244:245], v32 offset:12288\n"
    "v_mfma_f32_16x16x32_bf16 v[180:183], a[188:191], a[164:167], v[180:183]\n"
    "ds_read_b64_tr_b16 a[246:247], v32 offset:12800\n"
    "buffer_load_dwordx4 v21, s[8:11], s56 offen offset:0 lds\n"
    "v_mfma_f32_16x16x32_bf16 v[184:187], a[188:191], a[168:171], v[184:187]\n"
    "ds_read_b64_tr_b16 a[248:249], v33 offset:12288\n"
    "v_mfma_f32_16x16x32_bf16 v[188:191], a[188:191], a[172:175], v[188:191]\n"
    "ds_read_b64_tr_b16 a[250:251], v33 offset:12800\n"
    "buffer_load_dwordx4 v22, s[8:11], s56 offen offset:1024 lds\n"
    "v_mfma_f32_16x16x32_bf16 v[192:195], a[192:195], a[160:163], v[192:195]\n"
    "v_mfma_f32_16x16x32_bf16 v[196:199], a[192:195], a[164:167], v[196:199]\n"
    "v_mfma_f32_16x16x32_bf16 v[200:203], a[192:195], a[168:171], v[200:203]\n"
    "v_mfma_f32_16x16x32_bf16 v[204:207], a[192:195], a[172:175], v[204:207]\n"
    "v_mfma_f32_16x16x32_bf16 v[208:211], a[196:199], a[160:163], v[208:211]\n"
    "buffer_load_dwordx4 v23, s[8:11], s56 offen offset:2048 lds\n"
    "v_mfma_f32_16x16x32_bf16 v[212:215], a[196:199], a[164:167], v[212:215]\n"
    "v_mfma_f32_16x16x32_bf16 v[216:219], a[196:199], a[168:171], v[216:219]\n"
    "v_mfma_f32_16x16x32_bf16 v[220:223], a[196:199], a[172:175], v[220:223]\n"
    "v_mfma_f32_16x16x32_bf16 v[224:227], a[200:203], a[160:163], v[224:227]\n"
    "v_mfma_f32_16x16x32_bf16 v[228:231], a[200:203], a[164:167], v[228:231]\n"
    "v_mfma_f32_16x16x32_bf16 v[232:235], a[200:203], a[168:171], v[232:235]\n"
    "v_mfma_f32_16x16x32_bf16 v[236:239], a[200:203], a[172:175], v[236:239]\n"
    "s_waitcnt lgkmcnt(0)\n"
    "v_smfmac_f32_16x16x64_bf16 v[240:243], v[248:251], a[224:231], v252\n"
    "s_waitcnt vmcnt(11)\n"
    "s_barrier\n"
    "ds_read_b128 a[160:163], v25 offset:40960\n"
    "v_smfmac_f32_16x16x64_bf16 v[244:247], v[248:251], a[232:239], v252\n"
    "ds_read_b128 a[164:167], v25 offset:43008\n"
    "s_add_u32 s54, s54, 1\n"
    "s_and_b32 s54, s54, 15\n"
    "s_mul_i32 s56, s54, s29\n"
    "v_mfma_f32_16x16x32_bf16 v[128:131], a[224:227], a[208:211], v[128:131]\n"
    "ds_read_b128 a[168:171], v25 offset:45056\n"
    "v_mfma_f32_16x16x32_bf16 v[132:135], a[224:227], a[212:215], v[132:135]\n"
    "ds_read_b128 a[172:175], v25 offset:47104\n"
    "s_add_u32 m0, s60, 12288\n"
    "v_mfma_f32_16x16x32_bf16 v[136:139], a[224:227], a[216:219], v[136:139]\n"
    "ds_read_b64_tr_b16 a[176:177], v27 offset:24576\n"
    "v_mfma_f32_16x16x32_bf16 v[140:143], a[224:227], a[220:223], v[140:143]\n"
    "ds_read_b64_tr_b16 a[178:179], v27 offset:25088\n"
    "buffer_load_dwordx4 v21, s[8:11], s56 offen offset:0 lds\n"
    "v_mfma_f32_16x16x32_bf16 v[144:147], a[228:231], a[208:211], v[144:147]\n"
    "ds_read_b64_tr_b16 a[180:181], v28 offset:24576\n"
    "v_mfma_f32_16x16x32_bf16 v[148:151], a[228:231], a[212:215], v[148:151]\n"
    "ds_read_b64_tr_b16 a[182:183], v28 offset:25088\n"
    "buffer_load_dwordx4 v22, s[8:11], s56 offen offset:1024 lds\n"
    "v_mfma_f32_16x16x32_bf16 v[152:155], a[228:231], a[216:219], v[152:155]\n"
    "buffer_load_dwordx2 v[108:109], v34, s[16:19], 0 offen sc1\n"
    "ds_read_b64_tr_b16 a[184:185], v29 offset:24576\n"
    "v_mfma_f32_16x16x32_bf16 v[156:159], a[228:231], a[220:223], v[156:159]\n"
    "s_mul_i32 s48, s53, 14\n"
    "ds_read_b64_tr_b16 a[186:187], v29 offset:25088\n"
    "buffer_load_dwordx4 v23, s[8:11], s56 offen offset:2048 lds\n"
    "v_mfma_f32_16x16x32_bf16 v[160:163], a[232:235], a[208:211], v[160:163]\n"
    "s_add_u32 s49, s48, s26\n"
    "s_lshl_b32 s49, s49, 5\n"
    "ds_read_b64_tr_b16 a[188:189], v30 offset:40960\n"
    "v_mfma_f32_16x16x32_bf16 v[164:167], a[232:235], a[212:215], v[164:167]\n"
    "buffer_load_dwordx2 v[110:111], v124, s[20:23], s49 offen offset:0\n"
    "ds_read_b64_tr_b16 a[190:191], v30 offset:41216\n"
    "v_mfma_f32_16x16x32_bf16 v[168:171], a[232:235], a[216:219], v[168:171]\n"
    "s_add_u32 s55, s55, 1\n"
    "s_add_u32 s49, s48, s26\n"
    "s_lshl_b32 s49, s49, 5\n"
    "s_and_b32 s55, s55, 7\n"
    "s_lshl_b32 s57, s55, 7\n"
    "buffer_load_dwordx2 v[112:113], v124, s[20:23], s49 offen offset:32\n"
    "ds_read_b64_tr_b16 a[192:193], v31 offset:24576\n"
    "v_mfma_f32_16x16x32_bf16 v[172:175], a[232:235], a[220:223], v[172:175]\n"
    "ds_read_b64_tr_b16 a[194:195], v31 offset:25088\n"
    "s_add_u32 s49, s48, s26\n"
    "s_lshl_b32 s49, s49, 5\n"
    "v_mfma_f32_16x16x32_bf16 v[176:179], a[236:239], a[208:211], v[176:179]\n"
    "s_add_u32 m0, s59, 69632\n"
    "buffer_load_dwordx2 v[114:115], v124, s[20:23], s49 offen offset:64\n"
    "ds_read_b64_tr_b16 a[196:197], v32 offset:24576\n"
    "v_mfma_f32_16x16x32_bf16 v[180:183], a[236:239], a[212:215], v[180:183]\n"
    "ds_read_b64_tr_b16 a[198:199], v32 offset:25088\n"
    "s_add_u32 s49, s48, s30\n"
    "s_lshl_b32 s49, s49, 5\n"
    "v_mfma_f32_16x16x32_bf16 v[184:187], a[236:239], a[216:219], v[184:187]\n"
    "buffer_load_dwordx4 v17, s[4:7], s57 offen offset:0 lds\n"
    "buffer_load_dwordx2 v[116:117], v124, s[20:23], s49 offen offset:0\n"
    "ds_read_b64_tr_b16 a[200:201], v33 offset:24576\n"
    "v_mfma_f32_16x16x32_bf16 v[188:191], a[236:239], a[220:223], v[188:191]\n"
    "ds_read_b64_tr_b16 a[202:203], v33 offset:25088\n"
    "s_add_u32 s49, s48, s27\n"
    "s_lshl_b32 s49, s49, 5\n"
    "v_mfma_f32_16x16x32_bf16 v[192:195], a[240:243], a[208:211], v[192:195]\n"
    "buffer_load_dwordx4 v18, s[4:7], s57 offen offset:1024 lds\n"
    "buffer_load_dwordx2 v[118:119], v124, s[20:23], s49 offen offset:0\n"
    "v_mfma_f32_16x16x32_bf16 v[196:199], a[240:243], a[212:215], v[196:199]\n"
    "v_mfma_f32_16x16x32_bf16 v[200:203], a[240:243], a[216:219], v[200:203]\n"
    "v_mfma_f32_16x16x32_bf16 v[204:207], a[240:243], a[220:223], v[204:207]\n"
    "s_add_u32 s49, s48, s27\n"
    "s_lshl_b32 s49, s49, 5\n"
    "v_mfma_f32_16x16x32_bf16 v[208:211], a[244:247], a[208:211], v[208:211]\n"
    "buffer_load_dwordx4 v19, s[4:7], s57 offen offset:2048 lds\n"
    "buffer_load_dwordx2 v[120:121], v124, s[20:23], s49 offen offset:32\n"
    "v_mfma_f32_16x16x32_bf16 v[212:215], a[244:247], a[212:215], v[212:215]\n"
    "v_mfma_f32_16x16x32_bf16 v[216:219], a[244:247], a[216:219], v[216:219]\n"
    "v_mfma_f32_16x16x32_bf16 v[220:223], a[244:247], a[220:223], v[220:223]\n"
    "s_add_u32 s49, s48, s27\n"
    "s_lshl_b32 s49, s49, 5\n"
    "v_mfma_f32_16x16x32_bf16 v[224:227], a[248:251], a[208:211], v[224:227]\n"
    "v_mfma_f32_16x16x32_bf16 v[228:231], a[248:251], a[212:215], v[228:231]\n"
    "buffer_load_dwordx2 v[122:123], v124, s[20:23], s49 offen offset:64\n"
    "v_mfma_f32_16x16x32_bf16 v[232:235], a[248:251], a[216:219], v[232:235]\n"
    "v_mfma_f32_16x16x32_bf16 v[236:239], a[248:251], a[220:223], v[236:239]\n"
    "s_waitcnt lgkmcnt(0)\n"
    "v_smfmac_f32_16x16x64_bf16 v[240:243], v[248:251], a[176:183], v252\n"
    "s_waitcnt vmcnt(22)\n"
    "s_barrier\n"
    "ds_read_b128 a[208:211], v26 offset:40960\n"
    "v_smfmac_f32_16x16x64_bf16 v[244:247], v[248:251], a[184:191], v252\n"
    "ds_read_b128 a[212:215], v26 offset:43008\n"
    "s_add_u32 m0, s59, 69632\n"
    "s_mul_i32 s58, s55, s28\n"
    "v_mfma_f32_16x16x32_bf16 v[128:131], a[176:179], a[160:163], v[128:131]\n"
    "ds_read_b128 a[216:219], v26 offset:45056\n"
    "v_mfma_f32_16x16x32_bf16 v[132:135], a[176:179], a[164:167], v[132:135]\n"
    "ds_read_b128 a[220:223], v26 offset:47104\n"
    "buffer_load_dwordx4 v20, s[4:7], s57 offen offset:3072 lds\n"
    "v_mfma_f32_16x16x32_bf16 v[136:139], a[176:179], a[168:171], v[136:139]\n"
    "ds_read_b64_tr_b16 a[224:225], v27 offset:36864\n"
    "v_mfma_f32_16x16x32_bf16 v[140:143], a[176:179], a[172:175], v[140:143]\n"
    "ds_read_b64_tr_b16 a[226:227], v27 offset:37376\n"
    "v_mfma_f32_16x16x32_bf16 v[144:147], a[180:183], a[160:163], v[144:147]\n"
    "s_add_u32 m0, s61, 69632\n"
    "ds_read_b64_tr_b16 a[228:229], v28 offset:36864\n"
    "v_mfma_f32_16x16x32_bf16 v[148:151], a[180:183], a[164:167], v[148:151]\n"
    "ds_read_b64_tr_b16 a[230:231], v28 offset:37376\n"
    "v_mfma_f32_16x16x32_bf16 v[152:155], a[180:183], a[168:171], v[152:155]\n"
    "ds_read_b64_tr_b16 a[232:233], v29 offset:36864\n"
    "buffer_load_dwordx4 v24, s[36:39], s58 offen offset:0 lds\n"
    "v_mfma_f32_16x16x32_bf16 v[156:159], a[180:183], a[172:175], v[156:159]\n"
    "ds_read_b64_tr_b16 a[234:235], v29 offset:37376\n"
    "v_mfma_f32_16x16x32_bf16 v[160:163], a[184:187], a[160:163], v[160:163]\n"
    "ds_read_b64_tr_b16 a[236:237], v30 offset:43008\n"
    "s_add_u32 s54, s54, 1\n"
    "s_and_b32 s54, s54, 15\n"
    "s_mul_i32 s56, s54, s29\n"
    "v_mfma_f32_16x16x32_bf16 v[164:167], a[184:187], a[164:167], v[164:167]\n"
    "ds_read_b64_tr_b16 a[238:239], v30 offset:43264\n"
    "v_mfma_f32_16x16x32_bf16 v[168:171], a[184:187], a[168:171], v[168:171]\n"
    "ds_read_b64_tr_b16 a[240:241], v31 offset:36864\n"
    "v_mfma_f32_16x16x32_bf16 v[172:175], a[184:187], a[172:175], v[172:175]\n"
    "s_add_u32 m0, s60, 24576\n"
    "ds_read_b64_tr_b16 a[242:243], v31 offset:37376\n"
    "v_mfma_f32_16x16x32_bf16 v[176:179], a[188:191], a[160:163], v[176:179]\n"
    "ds_read_b64_tr_b16 a[244:245], v32 offset:36864\n"
    "v_mfma_f32_16x16x32_bf16 v[180:183], a[188:191], a[164:167], v[180:183]\n"
    "ds_read_b64_tr_b16 a[246:247], v32 offset:37376\n"
    "buffer_load_dwordx4 v21, s[8:11], s56 offen offset:0 lds\n"
    "v_mfma_f32_16x16x32_bf16 v[184:187], a[188:191], a[168:171], v[184:187]\n"
    "ds_read_b64_tr_b16 a[248:249], v33 offset:36864\n"
    "v_mfma_f32_16x16x32_bf16 v[188:191], a[188:191], a[172:175], v[188:191]\n"
    "ds_read_b64_tr_b16 a[250:251], v33 offset:37376\n"
    "buffer_load_dwordx4 v22, s[8:11], s56 offen offset:1024 lds\n"
    "v_mfma_f32_16x16x32_bf16 v[192:195], a[192:195], a[160:163], v[192:195]\n"
    "v_mfma_f32_16x16x32_bf16 v[196:199], a[192:195], a[164:167], v[196:199]\n"
    "v_mfma_f32_16x16x32_bf16 v[200:203], a[192:195], a[168:171], v[200:203]\n"
    "v_mfma_f32_16x16x32_bf16 v[204:207], a[192:195], a[172:175], v[204:207]\n"
    "v_mfma_f32_16x16x32_bf16 v[208:211], a[196:199], a[160:163], v[208:211]\n"
    "buffer_load_dwordx4 v23, s[8:11], s56 offen offset:2048 lds\n"
    "v_mfma_f32_16x16x32_bf16 v[212:215], a[196:199], a[164:167], v[212:215]\n"
    "v_mfma_f32_16x16x32_bf16 v[216:219], a[196:199], a[168:171], v[216:219]\n"
    "v_mfma_f32_16x16x32_bf16 v[220:223], a[196:199], a[172:175], v[220:223]\n"
    "v_mfma_f32_16x16x32_bf16 v[224:227], a[200:203], a[160:163], v[224:227]\n"
    "v_mfma_f32_16x16x32_bf16 v[228:231], a[200:203], a[164:167], v[228:231]\n"
    "v_mfma_f32_16x16x32_bf16 v[232:235], a[200:203], a[168:171], v[232:235]\n"
    "v_mfma_f32_16x16x32_bf16 v[236:239], a[200:203], a[172:175], v[236:239]\n"
    "s_waitcnt lgkmcnt(0)\n"
    "v_smfmac_f32_16x16x64_bf16 v[240:243], v[248:251], a[224:231], v252\n"
    "s_waitcnt vmcnt(19)\n"
    "s_barrier\n"
    "ds_read_b128 a[160:163], v25 offset:0\n"
    "v_smfmac_f32_16x16x64_bf16 v[244:247], v[248:251], a[232:239], v252\n"
    "ds_read_b128 a[164:167], v25 offset:2048\n"
    "s_add_u32 s54, s54, 1\n"
    "s_and_b32 s54, s54, 15\n"
    "s_mul_i32 s56, s54, s29\n"
    "v_mfma_f32_16x16x32_bf16 v[128:131], a[224:227], a[208:211], v[128:131]\n"
    "ds_read_b128 a[168:171], v25 offset:4096\n"
    "v_mfma_f32_16x16x32_bf16 v[132:135], a[224:227], a[212:215], v[132:135]\n"
    "ds_read_b128 a[172:175], v25 offset:6144\n"
    "v_mfma_f32_16x16x32_bf16 v[136:139], a[224:227], a[216:219], v[136:139]\n"
    "ds_read_b64_tr_b16 a[176:177], v27 offset:0\n"
    "v_mfma_f32_16x16x32_bf16 v[140:143], a[224:227], a[220:223], v[140:143]\n"
    "ds_read_b64_tr_b16 a[178:179], v27 offset:512\n"
    "s_add_u32 m0, s60, 36864\n"
    "v_mfma_f32_16x16x32_bf16 v[144:147], a[228:231], a[208:211], v[144:147]\n"
    "ds_read_b64_tr_b16 a[180:181], v28 offset:0\n"
    "v_mfma_f32_16x16x32_bf16 v[148:151], a[228:231], a[212:215], v[148:151]\n"
    "ds_read_b64_tr_b16 a[182:183], v28 offset:512\n"
    "v_mfma_f32_16x16x32_bf16 v[152:155], a[228:231], a[216:219], v[152:155]\n"
    "ds_read_b64_tr_b16 a[184:185], v29 offset:0\n"
    "v_mfma_f32_16x16x32_bf16 v[156:159], a[228:231], a[220:223], v[156:159]\n"
    "ds_read_b64_tr_b16 a[186:187], v29 offset:512\n"
    "buffer_load_dwordx4 v21, s[8:11], s56 offen offset:0 lds\n"
    "v_mfma_f32_16x16x32_bf16 v[160:163], a[232:235], a[208:211], v[160:163]\n"
    "ds_read_b64_tr_b16 a[188:189], v30 offset:0\n"
    "v_mfma_f32_16x16x32_bf16 v[164:167], a[232:235], a[212:215], v[164:167]\n"
    "ds_read_b64_tr_b16 a[190:191], v30 offset:256\n"
    "v_mfma_f32_16x16x32_bf16 v[168:171], a[232:235], a[216:219], v[168:171]\n"
    "ds_read_b64_tr_b16 a[192:193], v31 offset:0\n"
    "v_mfma_f32_16x16x32_bf16 v[172:175], a[232:235], a[220:223], v[172:175]\n"
    "ds_read_b64_tr_b16 a[194:195], v31 offset:512\n"
    "v_mfma_f32_16x16x32_bf16 v[176:179], a[236:239], a[208:211], v[176:179]\n"
    "buffer_load_dwordx4 v22, s[8:11], s56 offen offset:1024 lds\n"
    "ds_read_b64_tr_b16 a[196:197], v32 offset:0\n"
    "v_mfma_f32_16x16x32_bf16 v[180:183], a[236:239], a[212:215], v[180:183]\n"
    "ds_read_b64_tr_b16 a[198:199], v32 offset:512\n"
    "v_mfma_f32_16x16x32_bf16 v[184:187], a[236:239], a[216:219], v[184:187]\n"
    "ds_read_b64_tr_b16 a[200:201], v33 offset:0\n"
    "v_mfma_f32_16x16x32_bf16 v[188:191], a[236:239], a[220:223], v[188:191]\n"
    "ds_read_b64_tr_b16 a[202:203], v33 offset:512\n"
    "v_mfma_f32_16x16x32_bf16 v[192:195], a[240:243], a[208:211], v[192:195]\n"
    "buffer_load_dwordx4 v23, s[8:11], s56 offen offset:2048 lds\n"
    "v_mfma_f32_16x16x32_bf16 v[196:199], a[240:243], a[212:215], v[196:199]\n"
    "v_mfma_f32_16x16x32_bf16 v[200:203], a[240:243], a[216:219], v[200:203]\n"
    "v_mfma_f32_16x16x32_bf16 v[204:207], a[240:243], a[220:223], v[204:207]\n"
    "v_mfma_f32_16x16x32_bf16 v[208:211], a[244:247], a[208:211], v[208:211]\n"
    "v_mfma_f32_16x16x32_bf16 v[212:215], a[244:247], a[212:215], v[212:215]\n"
    "v_mfma_f32_16x16x32_bf16 v[216:219], a[244:247], a[216:219], v[216:219]\n"
    "v_mfma_f32_16x16x32_bf16 v[220:223], a[244:247], a[220:223], v[220:223]\n"
    "v_mfma_f32_16x16x32_bf16 v[224:227], a[248:251], a[208:211], v[224:227]\n"
    "v_mfma_f32_16x16x32_bf16 v[228:231], a[248:251], a[212:215], v[228:231]\n"
    "v_mfma_f32_16x16x32_bf16 v[232:235], a[248:251], a[216:219], v[232:235]\n"
    "v_mfma_f32_16x16x32_bf16 v[236:239], a[248:251], a[220:223], v[236:239]\n"
    "s_waitcnt lgkmcnt(0)\n"
    "v_smfmac_f32_16x16x64_bf16 v[240:243], v[248:251], a[176:183], v252\n"
    "s_waitcnt vmcnt(18)\n"
    "s_barrier\n"
    "ds_read_b128 a[208:211], v26 offset:0\n"
    "v_smfmac_f32_16x16x64_bf16 v[244:247], v[248:251], a[184:191], v252\n"
    "ds_read_b128 a[212:215], v26 offset:2048\n"
    "s_mov_b32 s50, 0\n"
    "s_add_u32 m0, s59, 114688\n"
    "v_mfma_f32_16x16x32_bf16 v[128:131], a[176:179], a[160:163], v[128:131]\n"
    "ds_read_b128 a[216:219], v26 offset:4096\n"
    "v_mfma_f32_16x16x32_bf16 v[132:135], a[176:179], a[164:167], v[132:135]\n"
    "ds_read_b128 a[220:223], v26 offset:6144\n"
    "v_mfma_f32_16x16x32_bf16 v[136:139], a[176:179], a[168:171], v[136:139]\n"
    "ds_read_b64_tr_b16 a[224:225], v27 offset:12288\n"
    "v_mfma_f32_16x16x32_bf16 v[140:143], a[176:179], a[172:175], v[140:143]\n"
    "ds_read_b64_tr_b16 a[226:227], v27 offset:12800\n"
    "buffer_load_dwordx4 v2, s[12:15], s50 offen offset:0 lds\n"
    "v_mfma_f32_16x16x32_bf16 v[144:147], a[180:183], a[160:163], v[144:147]\n"
    "ds_read_b64_tr_b16 a[228:229], v28 offset:12288\n"
    "v_mfma_f32_16x16x32_bf16 v[148:151], a[180:183], a[164:167], v[148:151]\n"
    "ds_read_b64_tr_b16 a[230:231], v28 offset:12800\n"
    "v_mfma_f32_16x16x32_bf16 v[152:155], a[180:183], a[168:171], v[152:155]\n"
    "ds_read_b64_tr_b16 a[232:233], v29 offset:12288\n"
    "v_mfma_f32_16x16x32_bf16 v[156:159], a[180:183], a[172:175], v[156:159]\n"
    "ds_read_b64_tr_b16 a[234:235], v29 offset:12800\n"
    "buffer_load_dwordx4 v3, s[12:15], s50 offen offset:1024 lds\n"
    "v_mfma_f32_16x16x32_bf16 v[160:163], a[184:187], a[160:163], v[160:163]\n"
    "ds_read_b64_tr_b16 a[236:237], v30 offset:2048\n"
    "v_mfma_f32_16x16x32_bf16 v[164:167], a[184:187], a[164:167], v[164:167]\n"
    "ds_read_b64_tr_b16 a[238:239], v30 offset:2304\n"
    "v_mfma_f32_16x16x32_bf16 v[168:171], a[184:187], a[168:171], v[168:171]\n"
    "ds_read_b64_tr_b16 a[240:241], v31 offset:12288\n"
    "v_mfma_f32_16x16x32_bf16 v[172:175], a[184:187], a[172:175], v[172:175]\n"
    "ds_read_b64_tr_b16 a[242:243], v31 offset:12800\n"
    "v_mfma_f32_16x16x32_bf16 v[176:179], a[188:191], a[160:163], v[176:179]\n"
    "buffer_load_dwordx4 v4, s[12:15], s50 offen offset:2048 lds\n"
    "ds_read_b64_tr_b16 a[244:245], v32 offset:12288\n"
    "v_mfma_f32_16x16x32_bf16 v[180:183], a[188:191], a[164:167], v[180:183]\n"
    "ds_read_b64_tr_b16 a[246:247], v32 offset:12800\n"
    "v_mfma_f32_16x16x32_bf16 v[184:187], a[188:191], a[168:171], v[184:187]\n"
    "ds_read_b64_tr_b16 a[248:249], v33 offset:12288\n"
    "v_mfma_f32_16x16x32_bf16 v[188:191], a[188:191], a[172:175], v[188:191]\n"
    "ds_read_b64_tr_b16 a[250:251], v33 offset:12800\n"
    "v_mfma_f32_16x16x32_bf16 v[192:195], a[192:195], a[160:163], v[192:195]\n"
    "buffer_load_dwordx4 v5, s[12:15], s50 offen offset:3072 lds\n"
    "v_mfma_f32_16x16x32_bf16 v[196:199], a[192:195], a[164:167], v[196:199]\n"
    "v_mfma_f32_16x16x32_bf16 v[200:203], a[192:195], a[168:171], v[200:203]\n"
    "v_mfma_f32_16x16x32_bf16 v[204:207], a[192:195], a[172:175], v[204:207]\n"
    "v_mfma_f32_16x16x32_bf16 v[208:211], a[196:199], a[160:163], v[208:211]\n"
    "v_mfma_f32_16x16x32_bf16 v[212:215], a[196:199], a[164:167], v[212:215]\n"
    "v_mfma_f32_16x16x32_bf16 v[216:219], a[196:199], a[168:171], v[216:219]\n"
    "v_mfma_f32_16x16x32_bf16 v[220:223], a[196:199], a[172:175], v[220:223]\n"
    "v_mfma_f32_16x16x32_bf16 v[224:227], a[200:203], a[160:163], v[224:227]\n"
    "v_mfma_f32_16x16x32_bf16 v[228:231], a[200:203], a[164:167], v[228:231]\n"
    "v_mfma_f32_16x16x32_bf16 v[232:235], a[200:203], a[168:171], v[232:235]\n"
    "v_mfma_f32_16x16x32_bf16 v[236:239], a[200:203], a[172:175], v[236:239]\n"
    "s_waitcnt lgkmcnt(0)\n"
    "v_smfmac_f32_16x16x64_bf16 v[240:243], v[248:251], a[224:231], v252\n"
    "s_waitcnt vmcnt(7)\n"
    "s_barrier\n"
    "ds_read_b128 a[160:163], v25 offset:20480\n"
    "v_smfmac_f32_16x16x64_bf16 v[244:247], v[248:251], a[232:239], v252\n"
    "ds_read_b128 a[164:167], v25 offset:22528\n"
    "s_mov_b32 s50, 32768\n"
    "s_add_u32 m0, s59, 131072\n"
    "v_mfma_f32_16x16x32_bf16 v[128:131], a[224:227], a[208:211], v[128:131]\n"
    "ds_read_b128 a[168:171], v25 offset:24576\n"
    "v_mfma_f32_16x16x32_bf16 v[132:135], a[224:227], a[212:215], v[132:135]\n"
    "ds_read_b128 a[172:175], v25 offset:26624\n"
    "v_mfma_f32_16x16x32_bf16 v[136:139], a[224:227], a[216:219], v[136:139]\n"
    "ds_read_b64_tr_b16 a[176:177], v27 offset:24576\n"
    "v_mfma_f32_16x16x32_bf16 v[140:143], a[224:227], a[220:223], v[140:143]\n"
    "ds_read_b64_tr_b16 a[178:179], v27 offset:25088\n"
    "buffer_load_dwordx4 v2, s[12:15], s50 offen offset:0 lds\n"
    "v_mfma_f32_16x16x32_bf16 v[144:147], a[228:231], a[208:211], v[144:147]\n"
    "ds_read_b64_tr_b16 a[180:181], v28 offset:24576\n"
    "v_mfma_f32_16x16x32_bf16 v[148:151], a[228:231], a[212:215], v[148:151]\n"
    "ds_read_b64_tr_b16 a[182:183], v28 offset:25088\n"
    "v_mfma_f32_16x16x32_bf16 v[152:155], a[228:231], a[216:219], v[152:155]\n"
    "s_waitcnt vmcnt(24)\n"
    "v_cmp_ne_u32 vcc, s66, v109\n"
    "s_nop 0\n"
    "s_cbranch_vccnz L_gre_ffn\n"
    "L_gok_ffn:\n"
    "ds_read_b64_tr_b16 a[184:185], v29 offset:24576\n"
    "v_mfma_f32_16x16x32_bf16 v[156:159], a[228:231], a[220:223], v[156:159]\n"
    "ds_read_b64_tr_b16 a[186:187], v29 offset:25088\n"
    "buffer_load_dwordx4 v3, s[12:15], s50 offen offset:1024 lds\n"
    "v_lshlrev_b32 v106, 16, v108\n"
    "v_mfma_f32_16x16x32_bf16 v[160:163], a[232:235], a[208:211], v[160:163]\n"
    "ds_read_b64_tr_b16 a[188:189], v30 offset:20480\n"
    "v_mfma_f32_16x16x32_bf16 v[164:167], a[232:235], a[212:215], v[164:167]\n"
    "ds_read_b64_tr_b16 a[190:191], v30 offset:20736\n"
    "v_and_b32 v107, 0xffff0000, v108\n"
    "v_mfma_f32_16x16x32_bf16 v[168:171], a[232:235], a[216:219], v[168:171]\n"
    "ds_read_b64_tr_b16 a[192:193], v31 offset:24576\n"
    "v_mfma_f32_16x16x32_bf16 v[172:175], a[232:235], a[220:223], v[172:175]\n"
    "ds_read_b64_tr_b16 a[194:195], v31 offset:25088\n"
    "v_mfma_f32_16x16x32_bf16 v[176:179], a[236:239], a[208:211], v[176:179]\n"
    "v_xor_b32 v106, 0x80000000, v106\n"
    "buffer_load_dwordx4 v4, s[12:15], s50 offen offset:2048 lds\n"
    "ds_read_b64_tr_b16 a[196:197], v32 offset:24576\n"
    "v_mfma_f32_16x16x32_bf16 v[180:183], a[236:239], a[212:215], v[180:183]\n"
    "ds_read_b64_tr_b16 a[198:199], v32 offset:25088\n"
    "v_rcp_f32 v107, v107\n"
    "v_mfma_f32_16x16x32_bf16 v[184:187], a[236:239], a[216:219], v[184:187]\n"
    "ds_read_b64_tr_b16 a[200:201], v33 offset:24576\n"
    "v_mfma_f32_16x16x32_bf16 v[188:191], a[236:239], a[220:223], v[188:191]\n"
    "ds_read_b64_tr_b16 a[202:203], v33 offset:25088\n"
    "v_lshlrev_b32 v105, 1, v35\n"
    "v_mfma_f32_16x16x32_bf16 v[192:195], a[240:243], a[208:211], v[192:195]\n"
    "buffer_load_dwordx4 v5, s[12:15], s50 offen offset:3072 lds\n"
    "v_mfma_f32_16x16x32_bf16 v[196:199], a[240:243], a[212:215], v[196:199]\n"
    "v_mfma_f32_16x16x32_bf16 v[200:203], a[240:243], a[216:219], v[200:203]\n"
    "v_mfma_f32_16x16x32_bf16 v[204:207], a[240:243], a[220:223], v[204:207]\n"
    "v_add_u32 v105, 110592, v105\n"
    "v_mfma_f32_16x16x32_bf16 v[208:211], a[244:247], a[208:211], v[208:211]\n"
    "v_mfma_f32_16x16x32_bf16 v[212:215], a[244:247], a[212:215], v[212:215]\n"
    "v_mfma_f32_16x16x32_bf16 v[216:219], a[244:247], a[216:219], v[216:219]\n"
    "v_mfma_f32_16x16x32_bf16 v[220:223], a[244:247], a[220:223], v[220:223]\n"
    "ds_write_b64 v105, v[106:107]\n"
    "v_mfma_f32_16x16x32_bf16 v[224:227], a[248:251], a[208:211], v[224:227]\n"
    "v_mfma_f32_16x16x32_bf16 v[228:231], a[248:251], a[212:215], v[228:231]\n"
    "v_mfma_f32_16x16x32_bf16 v[232:235], a[248:251], a[216:219], v[232:235]\n"
    "v_mfma_f32_16x16x32_bf16 v[236:239], a[248:251], a[220:223], v[236:239]\n"
    "s_waitcnt lgkmcnt(0)\n"
    "v_smfmac_f32_16x16x64_bf16 v[240:243], v[248:251], a[176:183], v252\n"
    "s_waitcnt vmcnt(8)\n"
    "s_barrier\n"
    "ds_read_b128 a[208:211], v26 offset:20480\n"
    "v_smfmac_f32_16x16x64_bf16 v[244:247], v[248:251], a[184:191], v252\n"
    "ds_read_b128 a[212:215], v26 offset:22528\n"
    "s_mov_b32 s50, 65536\n"
    "s_add_u32 m0, s59, 147456\n"
    "v_mfma_f32_16x16x32_bf16 v[128:131], a[176:179], a[160:163], v[128:131]\n"
    "ds_read_b128 a[216:219], v26 offset:24576\n"
    "v_mfma_f32_16x16x32_bf16 v[132:135], a[176:179], a[164:167], v[132:135]\n"
    "ds_read_b128 a[220:223], v26 offset:26624\n"
    "v_mfma_f32_16x16x32_bf16 v[136:139], a[176:179], a[168:171], v[136:139]\n"
    "ds_read_b64_tr_b16 a[224:225], v27 offset:36864\n"
    "v_mfma_f32_16x16x32_bf16 v[140:143], a[176:179], a[172:175], v[140:143]\n"
    "ds_read_b64_tr_b16 a[226:227], v27 offset:37376\n"
    "buffer_load_dwordx4 v2, s[12:15], s50 offen offset:0 lds\n"
    "v_mfma_f32_16x16x32_bf16 v[144:147], a[180:183], a[160:163], v[144:147]\n"
    "ds_read_b64_tr_b16 a[228:229], v28 offset:36864\n"
    "v_mfma_f32_16x16x32_bf16 v[148:151], a[180:183], a[164:167], v[148:151]\n"
    "ds_read_b64_tr_b16 a[230:231], v28 offset:37376\n"
    "v_mfma_f32_16x16x32_bf16 v[152:155], a[180:183], a[168:171], v[152:155]\n"
    "s_waitcnt vmcnt(17)\n"
    "ds_read_b64_tr_b16 a[232:233], v29 offset:36864\n"
    "v_mfma_f32_16x16x32_bf16 v[156:159], a[180:183], a[172:175], v[156:159]\n"
    "v_lshlrev_b32 v40, 16, v110\n"
    "v_and_b32 v41, 0xffff0000, v110\n"
    "ds_read_b64_tr_b16 a[234:235], v29 offset:37376\n"
    "buffer_load_dwordx4 v3, s[12:15], s50 offen offset:1024 lds\n"
    "v_lshlrev_b32 v42, 16, v111\n"
    "v_and_b32 v43, 0xffff0000, v111\n"
    "v_mfma_f32_16x16x32_bf16 v[160:163], a[184:187], a[160:163], v[160:163]\n"
    "ds_read_b64_tr_b16 a[236:237], v30 offset:22528\n"
    "v_mfma_f32_16x16x32_bf16 v[164:167], a[184:187], a[164:167], v[164:167]\n"
    "v_lshlrev_b32 v44, 16, v112\n"
    "v_and_b32 v45, 0xffff0000, v112\n"
    "ds_read_b64_tr_b16 a[238:239], v30 offset:22784\n"
    "v_mfma_f32_16x16x32_bf16 v[168:171], a[184:187], a[168:171], v[168:171]\n"
    "v_lshlrev_b32 v46, 16, v113\n"
    "v_and_b32 v47, 0xffff0000, v113\n"
    "ds_read_b64_tr_b16 a[240:241], v31 offset:36864\n"
    "v_mfma_f32_16x16x32_bf16 v[172:175], a[184:187], a[172:175], v[172:175]\n"
    "v_lshlrev_b32 v48, 16, v114\n"
    "v_and_b32 v49, 0xffff0000, v114\n"
    "ds_read_b64_tr_b16 a[242:243], v31 offset:37376\n"
    "v_mfma_f32_16x16x32_bf16 v[176:179], a[188:191], a[160:163], v[176:179]\n"
    "buffer_load_dwordx4 v4, s[12:15], s50 offen offset:2048 lds\n"
    "v_lshlrev_b32 v50, 16, v115\n"
    "v_and_b32 v51, 0xffff0000, v115\n"
    "ds_read_b64_tr_b16 a[244:245], v32 offset:36864\n"
    "v_mfma_f32_16x16x32_bf16 v[180:183], a[188:191], a[164:167], v[180:183]\n"
    "v_lshlrev_b32 v52, 16, v116\n"
    "v_and_b32 v53, 0xffff0000, v116\n"
    "ds_read_b64_tr_b16 a[246:247], v32 offset:37376\n"
    "v_mfma_f32_16x16x32_bf16 v[184:187], a[188:191], a[168:171], v[184:187]\n"
    "v_lshlrev_b32 v54, 16, v117\n"
    "v_and_b32 v55, 0xffff0000, v117\n"
    "ds_read_b64_tr_b16 a[248:249], v33 offset:36864\n"
    "v_mfma_f32_16x16x32_bf16 v[188:191], a[188:191], a[172:175], v[188:191]\n"
    "ds_read_b64_tr_b16 a[250:251], v33 offset:37376\n"
    "v_lshlrev_b32 v56, 16, v118\n"
    "v_and_b32 v57, 0xffff0000, v118\n"
    "v_mfma_f32_16x16x32_bf16 v[192:195], a[192:195], a[160:163], v[192:195]\n"
    "buffer_load_dwordx4 v5, s[12:15], s50 offen offset:3072 lds\n"
    "v_lshlrev_b32 v58, 16, v119\n"
    "v_and_b32 v59, 0xffff0000, v119\n"
    "v_mfma_f32_16x16x32_bf16 v[196:199], a[192:195], a[164:167], v[196:199]\n"
    "v_mfma_f32_16x16x32_bf16 v[200:203], a[192:195], a[168:171], v[200:203]\n"
    "v_lshlrev_b32 v60, 16, v120\n"
    "v_and_b32 v61, 0xffff0000, v120\n"
    "v_mfma_f32_16x16x32_bf16 v[204:207], a[192:195], a[172:175], v[204:207]\n"
    "v_mfma_f32_16x16x32_bf16 v[208:211], a[196:199], a[160:163], v[208:211]\n"
    "v_mfma_f32_16x16x32_bf16 v[212:215], a[196:199], a[164:167], v[212:215]\n"
    "v_lshlrev_b32 v62, 16, v121\n"
    "v_and_b32 v63, 0xffff0000, v121\n"
    "v_mfma_f32_16x16x32_bf16 v[216:219], a[196:199], a[168:171], v[216:219]\n"
    "v_mfma_f32_16x16x32_bf16 v[220:223], a[196:199], a[172:175], v[220:223]\n"
    "v_lshlrev_b32 v64, 16, v122\n"
    "v_and_b32 v65, 0xffff0000, v122\n"
    "v_mfma_f32_16x16x32_bf16 v[224:227], a[200:203], a[160:163], v[224:227]\n"
    "v_mfma_f32_16x16x32_bf16 v[228:231], a[200:203], a[164:167], v[228:231]\n"
    "v_lshlrev_b32 v66, 16, v123\n"
    "v_and_b32 v67, 0xffff0000, v123\n"
    "v_mfma_f32_16x16x32_bf16 v[232:235], a[200:203], a[168:171], v[232:235]\n"
    "v_mfma_f32_16x16x32_bf16 v[236:239], a[200:203], a[172:175], v[236:239]\n"
    "s_waitcnt lgkmcnt(0)\n"
    "v_smfmac_f32_16x16x64_bf16 v[240:243], v[248:251], a[224:231], v252\n"
    "s_barrier\n"
    "v_smfmac_f32_16x16x64_bf16 v[244:247], v[248:251], a[232:239], v252\n"
    "v_mfma_f32_16x16x32_bf16 v[128:131], a[224:227], a[208:211], v[128:131]\n"
    "s_mov_b32 s50, 98304\n"
    "s_add_u32 m0, s59, 0\n"
    "v_mfma_f32_16x16x32_bf16 v[132:135], a[224:227], a[212:215], v[132:135]\n"
    "v_mfma_f32_16x16x32_bf16 v[136:139], a[224:227], a[216:219], v[136:139]\n"
    "ds_read_b64 v[96:97], v36 offset:0\n"
    "ds_read_b64 v[98:99], v36 offset:128\n"
    "v_mfma_f32_16x16x32_bf16 v[140:143], a[224:227], a[220:223], v[140:143]\n"
    "ds_read_b64 v[100:101], v36 offset:256\n"
    "v_mfma_f32_16x16x32_bf16 v[144:147], a[228:231], a[208:211], v[144:147]\n"
    "ds_read_b64 v[102:103], v36 offset:384\n"
    "v_mfma_f32_16x16x32_bf16 v[148:151], a[228:231], a[212:215], v[148:151]\n"
    "v_add_f32 v104, v240, v241\n"
    "buffer_load_dwordx4 v2, s[12:15], s50 offen offset:0 lds\n"
    "v_mfma_f32_16x16x32_bf16 v[152:155], a[228:231], a[216:219], v[152:155]\n"
    "v_add_f32 v105, v242, v243\n"
    "v_mfma_f32_16x16x32_bf16 v[156:159], a[228:231], a[220:223], v[156:159]\n"
    "v_add_f32 v106, v244, v245\n"
    "v_mfma_f32_16x16x32_bf16 v[160:163], a[232:235], a[208:211], v[160:163]\n"
    "v_add_f32 v107, v246, v247\n"
    "v_mfma_f32_16x16x32_bf16 v[164:167], a[232:235], a[212:215], v[164:167]\n"
    "s_lshl_b32 s48, s26, 6\n"
    "v_add_u32 v108, s48, v38\n"
    "v_mfma_f32_16x16x32_bf16 v[168:171], a[232:235], a[216:219], v[168:171]\n"
    "s_lshl_b32 s49, s30, 6\n"
    "buffer_load_dwordx4 v3, s[12:15], s50 offen offset:1024 lds\n"
    "v_add_u32 v109, s49, v38\n"
    "v_mfma_f32_16x16x32_bf16 v[172:175], a[232:235], a[220:223], v[172:175]\n"
    "ds_write_b32 v108, v104 offset:0\n"
    "v_mfma_f32_16x16x32_bf16 v[176:179], a[236:239], a[208:211], v[176:179]\n"
    "ds_write_b32 v108, v105 offset:64\n"
    "v_mfma_f32_16x16x32_bf16 v[180:183], a[236:239], a[212:215], v[180:183]\n"
    "ds_write_b32 v108, v106 offset:128\n"
    "v_mfma_f32_16x16x32_bf16 v[184:187], a[236:239], a[216:219], v[184:187]\n"
    "ds_write_b32 v109, v107\n"
    "v_mfma_f32_16x16x32_bf16 v[188:191], a[236:239], a[220:223], v[188:191]\n"
    "v_add_u32 v108, s48, v37\n"
    "buffer_load_dwordx4 v4, s[12:15], s50 offen offset:2048 lds\n"
    "v_mfma_f32_16x16x32_bf16 v[192:195], a[240:243], a[208:211], v[192:195]\n"
    "v_add_u32 v109, s49, v37\n"
    "v_mfma_f32_16x16x32_bf16 v[196:199], a[240:243], a[212:215], v[196:199]\n"
    "s_lshl_b32 s48, s27, 6\n"
    "v_add_u32 v110, s48, v37\n"
    "v_mfma_f32_16x16x32_bf16 v[200:203], a[240:243], a[216:219], v[200:203]\n"
    "s_waitcnt lgkmcnt(0)\n"
    "s_barrier\n"
    "v_mfma_f32_16x16x32_bf16 v[204:207], a[240:243], a[220:223], v[204:207]\n"
    "ds_read_b128 v[68:71], v108 offset:2304\n"
    "v_mfma_f32_16x16x32_bf16 v[208:211], a[244:247], a[208:211], v[208:211]\n"
    "buffer_load_dwordx4 v5, s[12:15], s50 offen offset:3072 lds\n"
    "ds_read_b128 v[72:75], v108 offset:2368\n"
    "v_mfma_f32_16x16x32_bf16 v[212:215], a[244:247], a[212:215], v[212:215]\n"
    "ds_read_b128 v[76:79], v108 offset:2432\n"
    "v_mfma_f32_16x16x32_bf16 v[216:219], a[244:247], a[216:219], v[216:219]\n"
    "ds_read_b128 v[80:83], v109 offset:2304\n"
    "v_mfma_f32_16x16x32_bf16 v[220:223], a[244:247], a[220:223], v[220:223]\n"
    "ds_read_b128 v[84:87], v110 offset:2304\n"
    "v_mfma_f32_16x16x32_bf16 v[224:227], a[248:251], a[208:211], v[224:227]\n"
    "ds_read_b128 v[88:91], v110 offset:2368\n"
    "v_mfma_f32_16x16x32_bf16 v[228:231], a[248:251], a[212:215], v[228:231]\n"
    "ds_read_b128 v[92:95], v110 offset:2432\n"
    "v_mfma_f32_16x16x32_bf16 v[232:235], a[248:251], a[216:219], v[232:235]\n"
    "v_mfma_f32_16x16x32_bf16 v[236:239], a[248:251], a[220:223], v[236:239]\n"
    "s_waitcnt lgkmcnt(0)\n"
    "s_lshr_b32 s48, s3, 1\n"
    "s_mul_i32 s48, s48, 288\n"
    "s_mul_i32 s49, s53, 14\n"
    "s_add_u32 s48, s48, s49\n"
    "s_add_u32 s48, s48, 32\n"
    "s_add_u32 s26, s26, s48\n"
    "s_add_u32 s27, s27, s48\n"
    "s_add_u32 s30, s30, s48\n"
    "s_lshl_b32 s26, s26, 10\n"
    "s_lshl_b32 s27, s27, 10\n"
    "s_lshl_b32 s30, s30, 10\n"
    "v_pk_fma_f32 v[128:129], v[68:69], v[96:97], v[128:129] op_sel_hi:[1,0,1]\n"
    "s_waitcnt vmcnt(12)\n"
    "v_pk_fma_f32 v[130:131], v[70:71], v[96:97], v[130:131] op_sel_hi:[1,0,1]\n"
    "v_pk_fma_f32 v[132:133], v[68:69], v[98:99], v[132:133] op_sel_hi:[1,0,1]\n"
    "v_pk_fma_f32 v[134:135], v[70:71], v[98:99], v[134:135] op_sel_hi:[1,0,1]\n"
    "v_pk_fma_f32 v[128:129], v[128:129], v[96:97], v[40:41] op_sel:[0,1,0]\n"
    "v_pk_fma_f32 v[130:131], v[130:131], v[96:97], v[42:43] op_sel:[0,1,0]\n"
    "v_pk_fma_f32 v[132:133], v[132:133], v[98:99], v[40:41] op_sel:[0,1,0]\n"
    "v_pk_fma_f32 v[134:135], v[134:135], v[98:99], v[42:43] op_sel:[0,1,0]\n"
    "v_mul_f32_e64 v128, v128, |v128|\n"
    "v_mul_f32_e64 v129, v129, |v129|\n"
    "v_mul_f32_e64 v130, v130, |v130|\n"
    "v_mul_f32_e64 v131, v131, |v131|\n"
    "v_mul_f32_e64 v132, v132, |v132|\n"
    "v_mul_f32_e64 v133, v133, |v133|\n"
    "v_mul_f32_e64 v134, v134, |v134|\n"
    "v_mul_f32_e64 v135, v135, |v135|\n"
    "ds_read_b64_tr_b16 a[0:1], v10 offset:16384\n"
    "ds_read_b64_tr_b16 a[2:3], v10 offset:18432\n"
    "v_cvt_pk_bf16_f32 v128, v128, v129\n"
    "v_cvt_pk_bf16_f32 v129, v130, v131\n"
    "v_cvt_pk_bf16_f32 v130, v132, v133\n"
    "v_cvt_pk_bf16_f32 v131, v134, v135\n"
    "v_pk_max_i16 v128, v128, 0\n"
    "v_pk_max_i16 v129, v129, 0\n"
    "v_pk_max_i16 v130, v130, 0\n"
    "v_pk_max_i16 v131, v131, 0\n"
    "s_add_u32 s49, s26, 0\n"
    "s_add_u32 s49, s49, s64\n"
    "buffer_store_dwordx4 v[128:131], v14, s[16:19], s49 offen sc1\n"
    "v_pk_fma_f32 v[136:137], v[68:69], v[100:101], v[136:137] op_sel_hi:[1,0,1]\n"
    "v_pk_fma_f32 v[138:139], v[70:71], v[100:101], v[138:139] op_sel_hi:[1,0,1]\n"
    "v_pk_fma_f32 v[140:141], v[68:69], v[102:103], v[140:141] op_sel_hi:[1,0,1]\n"
    "v_pk_fma_f32 v[142:143], v[70:71], v[102:103], v[142:143] op_sel_hi:[1,0,1]\n"
    "ds_read_b64_tr_b16 a[4:5], v11 offset:16384\n"
    "ds_read_b64_tr_b16 a[6:7], v11 offset:18432\n"
    "v_pk_fma_f32 v[136:137], v[136:137], v[100:101], v[40:41] op_sel:[0,1,0]\n"
    "v_pk_fma_f32 v[138:139], v[138:139], v[100:101], v[42:43] op_sel:[0,1,0]\n"
    "v_pk_fma_f32 v[140:141], v[140:141], v[102:103], v[40:41] op_sel:[0,1,0]\n"
    "v_pk_fma_f32 v[142:143], v[142:143], v[102:103], v[42:43] op_sel:[0,1,0]\n"
    "v_mul_f32_e64 v136, v136, |v136|\n"
    "v_mul_f32_e64 v137, v137, |v137|\n"
    "v_mul_f32_e64 v138, v138, |v138|\n"
    "v_mul_f32_e64 v139, v139, |v139|\n"
    "v_mul_f32_e64 v140, v140, |v140|\n"
    "v_mul_f32_e64 v141, v141, |v141|\n"
    "v_mul_f32_e64 v142, v142, |v142|\n"
    "v_mul_f32_e64 v143, v143, |v143|\n"
    "v_cvt_pk_bf16_f32 v136, v136, v137\n"
    "v_cvt_pk_bf16_f32 v137, v138, v139\n"
    "v_cvt_pk_bf16_f32 v138, v140, v141\n"
    "ds_read_b64_tr_b16 a[8:9], v12 offset:16384\n"
    "ds_read_b64_tr_b16 a[10:11], v12 offset:18432\n"
    "v_cvt_pk_bf16_f32 v139, v142, v143\n"
    "v_pk_max_i16 v136, v136, 0\n"
    "v_pk_max_i16 v137, v137, 0\n"
    "v_pk_max_i16 v138, v138, 0\n"
    "v_pk_max_i16 v139, v139, 0\n"
    "s_add_u32 s49, s26, 147456\n"
    "s_add_u32 s49, s49, s64\n"
    "buffer_store_dwordx4 v[136:139], v14, s[16:19], s49 offen sc1\n"
    "v_pk_fma_f32 v[144:145], v[72:73], v[96:97], v[144:145] op_sel_hi:[1,0,1]\n"
    "v_pk_fma_f32 v[146:147], v[74:75], v[96:97], v[146:147] op_sel_hi:[1,0,1]\n"
    "v_pk_fma_f32 v[148:149], v[72:73], v[98:99], v[148:149] op_sel_hi:[1,0,1]\n"
    "v_pk_fma_f32 v[150:151], v[74:75], v[98:99], v[150:151] op_sel_hi:[1,0,1]\n"
    "v_pk_fma_f32 v[144:145], v[144:145], v[96:97], v[44:45] op_sel:[0,1,0]\n"
    "v_pk_fma_f32 v[146:147], v[146:147], v[96:97], v[46:47] op_sel:[0,1,0]\n"
    "v_pk_fma_f32 v[148:149], v[148:149], v[98:99], v[44:45] op_sel:[0,1,0]\n"
    "ds_read_b64_tr_b16 a[12:13], v13 offset:16384\n"
    "ds_read_b64_tr_b16 a[14:15], v13 offset:18432\n"
    "v_pk_fma_f32 v[150:151], v[150:151], v[98:99], v[46:47] op_sel:[0,1,0]\n"
    "v_mul_f32_e64 v144, v144, |v144|\n"
    "v_mul_f32_e64 v145, v145, |v145|\n"
    "v_mul_f32_e64 v146, v146, |v146|\n"
    "v_mul_f32_e64 v147, v147, |v147|\n"
    "v_mul_f32_e64 v148, v148, |v148|\n"
    "v_mul_f32_e64 v149, v149, |v149|\n"
    "v_mul_f32_e64 v150, v150, |v150|\n"
    "v_mul_f32_e64 v151, v151, |v151|\n"
    "v_cvt_pk_bf16_f32 v144, v144, v145\n"
    "v_cvt_pk_bf16_f32 v145, v146, v147\n"
    "v_cvt_pk_bf16_f32 v146, v148, v149\n"
    "v_cvt_pk_bf16_f32 v147, v150, v151\n"
    "v_pk_max_i16 v144, v144, 0\n"
    "v_pk_max_i16 v145, v145, 0\n"
    "s_mov_b32 s50, 131072\n"
    "s_add_u32 m0, s59, 49152\n"
    "v_pk_max_i16 v146, v146, 0\n"
    "v_pk_max_i16 v147, v147, 0\n"
    "s_add_u32 s49, s26, 1024\n"
    "s_add_u32 s49, s49, s64\n"
    "buffer_store_dwordx4 v[144:147], v14, s[16:19], s49 offen sc1\n"
    "v_pk_fma_f32 v[152:153], v[72:73], v[100:101], v[152:153] op_sel_hi:[1,0,1]\n"
    "v_pk_fma_f32 v[154:155], v[74:75], v[100:101], v[154:155] op_sel_hi:[1,0,1]\n"
    "v_pk_fma_f32 v[156:157], v[72:73], v[102:103], v[156:157] op_sel_hi:[1,0,1]\n"
    "v_pk_fma_f32 v[158:159], v[74:75], v[102:103], v[158:159] op_sel_hi:[1,0,1]\n"
    "v_pk_fma_f32 v[152:153], v[152:153], v[100:101], v[44:45] op_sel:[0,1,0]\n"
    "v_pk_fma_f32 v[154:155], v[154:155], v[100:101], v[46:47] op_sel:[0,1,0]\n"
    "v_pk_fma_f32 v[156:157], v[156:157], v[102:103], v[44:45] op_sel:[0,1,0]\n"
    "v_pk_fma_f32 v[158:159], v[158:159], v[102:103], v[46:47] op_sel:[0,1,0]\n"
    "v_mul_f32_e64 v152, v152, |v152|\n"
    "v_mul_f32_e64 v153, v153, |v153|\n"
    "buffer_load_dwordx4 v2, s[12:15], s50 offen offset:0 lds\n"
    "v_mul_f32_e64 v154, v154, |v154|\n"
    "v_mul_f32_e64 v155, v155, |v155|\n"
    "v_mul_f32_e64 v156, v156, |v156|\n"
    "v_mul_f32_e64 v157, v157, |v157|\n"
    "v_mul_f32_e64 v158, v158, |v158|\n"
    "v_mul_f32_e64 v159, v159, |v159|\n"
    "v_cvt_pk_bf16_f32 v152, v152, v153\n"
    "v_cvt_pk_bf16_f32 v153, v154, v155\n"
    "v_cvt_pk_bf16_f32 v154, v156, v157\n"
    "v_cvt_pk_bf16_f32 v155, v158, v159\n"
    "v_pk_max_i16 v152, v152, 0\n"
    "v_pk_max_i16 v153, v153, 0\n"
    "v_pk_max_i16 v154, v154, 0\n"
    "v_pk_max_i16 v155, v155, 0\n"
    "s_add_u32 s49, s26, 148480\n"
    "buffer_load_dwordx4 v3, s[12:15], s50 offen offset:1024 lds\n"
    "s_add_u32 s49, s49, s64\n"
    "buffer_store_dwordx4 v[152:155], v14, s[16:19], s49 offen sc1\n"
    "v_pk_fma_f32 v[160:161], v[76:77], v[96:97], v[160:161] op_sel_hi:[1,0,1]\n"
    "v_pk_fma_f32 v[162:163], v[78:79], v[96:97], v[162:163] op_sel_hi:[1,0,1]\n"
    "v_pk_fma_f32 v[164:165], v[76:77], v[98:99], v[164:165] op_sel_hi:[1,0,1]\n"
    "v_pk_fma_f32 v[166:167], v[78:79], v[98:99], v[166:167] op_sel_hi:[1,0,1]\n"
    "v_pk_fma_f32 v[160:161], v[160:161], v[96:97], v[48:49] op_sel:[0,1,0]\n"
    "v_pk_fma_f32 v[162:163], v[162:163], v[96:97], v[50:51] op_sel:[0,1,0]\n"
    "v_pk_fma_f32 v[164:165], v[164:165], v[98:99], v[48:49] op_sel:[0,1,0]\n"
    "v_pk_fma_f32 v[166:167], v[166:167], v[98:99], v[50:51] op_sel:[0,1,0]\n"
    "v_mul_f32_e64 v160, v160, |v160|\n"
    "v_mul_f32_e64 v161, v161, |v161|\n"
    "v_mul_f32_e64 v162, v162, |v162|\n"
    "v_mul_f32_e64 v163, v163, |v163|\n"
    "v_mul_f32_e64 v164, v164, |v164|\n"
    "buffer_load_dwordx4 v4, s[12:15], s50 offen offset:2048 lds\n"
    "v_mul_f32_e64 v165, v165, |v165|\n"
    "v_mul_f32_e64 v166, v166, |v166|\n"
    "v_mul_f32_e64 v167, v167, |v167|\n"
    "v_cvt_pk_bf16_f32 v160, v160, v161\n"
    "v_cvt_pk_bf16_f32 v161, v162, v163\n"
    "v_cvt_pk_bf16_f32 v162, v164, v165\n"
    "v_cvt_pk_bf16_f32 v163, v166, v167\n"
    "v_pk_max_i16 v160, v160, 0\n"
    "v_pk_max_i16 v161, v161, 0\n"
    "v_pk_max_i16 v162, v162, 0\n"
    "v_pk_max_i16 v163, v163, 0\n"
    "s_add_u32 s49, s26, 2048\n"
    "s_add_u32 s49, s49, s64\n"
    "buffer_store_dwordx4 v[160:163], v14, s[16:19], s49 offen sc1\n"
    "v_pk_fma_f32 v[168:169], v[76:77], v[100:101], v[168:169] op_sel_hi:[1,0,1]\n"
    "v_pk_fma_f32 v[170:171], v[78:79], v[100:101], v[170:171] op_sel_hi:[1,0,1]\n"
    "buffer_load_dwordx4 v5, s[12:15], s50 offen offset:3072 lds\n"
    "v_pk_fma_f32 v[172:173], v[76:77], v[102:103], v[172:173] op_sel_hi:[1,0,1]\n"
    "v_pk_fma_f32 v[174:175], v[78:79], v[102:103], v[174:175] op_sel_hi:[1,0,1]\n"
    "v_pk_fma_f32 v[168:169], v[168:169], v[100:101], v[48:49] op_sel:[0,1,0]\n"
    "v_pk_fma_f32 v[170:171], v[170:171], v[100:101], v[50:51] op_sel:[0,1,0]\n"
    "v_pk_fma_f32 v[172:173], v[172:173], v[102:103], v[48:49] op_sel:[0,1,0]\n"
    "v_pk_fma_f32 v[174:175], v[174:175], v[102:103], v[50:51] op_sel:[0,1,0]\n"
    "v_mul_f32_e64 v168, v168, |v168|\n"
    "v_mul_f32_e64 v169, v169, |v169|\n"
    "v_mul_f32_e64 v170, v170, |v170|\n"
    "v_mul_f32_e64 v171, v171, |v171|\n"
    "v_mul_f32_e64 v172, v172, |v172|\n"
    "v_mul_f32_e64 v173, v173, |v173|\n"
    "v_mul_f32_e64 v174, v174, |v174|\n"
    "v_mul_f32_e64 v175, v175, |v175|\n"
    "v_cvt_pk_bf16_f32 v168, v168, v169\n"
    "s_waitcnt vmcnt(17)\n"
    "v_cvt_pk_bf16_f32 v169, v170, v171\n"
    "v_cvt_pk_bf16_f32 v170, v172, v173\n"
    "v_cvt_pk_bf16_f32 v171, v174, v175\n"
    "v_pk_max_i16 v168, v168, 0\n"
    "v_pk_max_i16 v169, v169, 0\n"
    "v_pk_max_i16 v170, v170, 0\n"
    "v_pk_max_i16 v171, v171, 0\n"
    "s_add_u32 s49, s26, 149504\n"
    "s_add_u32 s49, s49, s64\n"
    "buffer_store_dwordx4 v[168:171], v14, s[16:19], s49 offen sc1\n"
    "v_pk_fma_f32 v[176:177], v[80:81], v[96:97], v[176:177] op_sel_hi:[1,0,1]\n"
    "v_pk_fma_f32 v[178:179], v[82:83], v[96:97], v[178:179] op_sel_hi:[1,0,1]\n"
    "v_pk_fma_f32 v[180:181], v[80:81], v[98:99], v[180:181] op_sel_hi:[1,0,1]\n"
    "v_pk_fma_f32 v[182:183], v[82:83], v[98:99], v[182:183] op_sel_hi:[1,0,1]\n"
    "v_pk_fma_f32 v[176:177], v[176:177], v[96:97], v[52:53] op_sel:[0,1,0]\n"
    "ds_read_b64_tr_b16 a[16:17], v10 offset:32768\n"
    "ds_read_b64_tr_b16 a[18:19], v10 offset:34816\n"
    "v_pk_fma_f32 v[178:179], v[178:179], v[96:97], v[54:55] op_sel:[0,1,0]\n"
    "v_pk_fma_f32 v[180:181], v[180:181], v[98:99], v[52:53] op_sel:[0,1,0]\n"
    "v_pk_fma_f32 v[182:183], v[182:183], v[98:99], v[54:55] op_sel:[0,1,0]\n"
    "v_mul_f32_e64 v176, v176, |v176|\n"
    "v_mul_f32_e64 v177, v177, |v177|\n"
    "v_mul_f32_e64 v178, v178, |v178|\n"
    "v_mul_f32_e64 v179, v179, |v179|\n"
    "v_mul_f32_e64 v180, v180, |v180|\n"
    "v_mul_f32_e64 v181, v181, |v181|\n"
    "v_mul_f32_e64 v182, v182, |v182|\n"
    "v_mul_f32_e64 v183, v183, |v183|\n"
    "v_cvt_pk_bf16_f32 v176, v176, v177\n"
    "v_cvt_pk_bf16_f32 v177, v178, v179\n"
    "v_cvt_pk_bf16_f32 v178, v180, v181\n"
    "v_cvt_pk_bf16_f32 v179, v182, v183\n"
    "ds_read_b64_tr_b16 a[20:21], v11 offset:32768\n"
    "ds_read_b64_tr_b16 a[22:23], v11 offset:34816\n"
    "v_pk_max_i16 v176, v176, 0\n"
    "v_pk_max_i16 v177, v177, 0\n"
    "v_pk_max_i16 v178, v178, 0\n"
    "v_pk_max_i16 v179, v179, 0\n"
    "s_add_u32 s49, s30, 0\n"
    "s_add_u32 s49, s49, s64\n"
    "buffer_store_dwordx4 v[176:179], v14, s[16:19], s49 offen sc1\n"
    "v_pk_fma_f32 v[184:185], v[80:81], v[100:101], v[184:185] op_sel_hi:[1,0,1]\n"
    "v_pk_fma_f32 v[186:187], v[82:83], v[100:101], v[186:187] op_sel_hi:[1,0,1]\n"
    "v_pk_fma_f32 v[188:189], v[80:81], v[102:103], v[188:189] op_sel_hi:[1,0,1]\n"
    "v_pk_fma_f32 v[190:191], v[82:83], v[102:103], v[190:191] op_sel_hi:[1,0,1]\n"
    "v_pk_fma_f32 v[184:185], v[184:185], v[100:101], v[52:53] op_sel:[0,1,0]\n"
    "v_pk_fma_f32 v[186:187], v[186:187], v[100:101], v[54:55] op_sel:[0,1,0]\n"
    "v_pk_fma_f32 v[188:189], v[188:189], v[102:103], v[52:53] op_sel:[0,1,0]\n"
    "v_pk_fma_f32 v[190:191], v[190:191], v[102:103], v[54:55] op_sel:[0,1,0]\n"
    "ds_read_b64_tr_b16 a[24:25], v12 offset:32768\n"
    "ds_read_b64_tr_b16 a[26:27], v12 offset:34816\n"
    "v_mul_f32_e64 v184, v184, |v184|\n"
    "v_mul_f32_e64 v185, v185, |v185|\n"
    "v_mul_f32_e64 v186, v186, |v186|\n"
    "v_mul_f32_e64 v187, v187, |v187|\n"
    "v_mul_f32_e64 v188, v188, |v188|\n"
    "v_mul_f32_e64 v189, v189, |v189|\n"
    "v_mul_f32_e64 v190, v190, |v190|\n"
    "v_mul_f32_e64 v191, v191, |v191|\n"
    "v_cvt_pk_bf16_f32 v184, v184, v185\n"
    "v_cvt_pk_bf16_f32 v185, v186, v187\n"
    "v_cvt_pk_bf16_f32 v186, v188, v189\n"
    "v_cvt_pk_bf16_f32 v187, v190, v191\n"
    "v_pk_max_i16 v184, v184, 0\n"
    "v_pk_max_i16 v185, v185, 0\n"
    "v_pk_max_i16 v186, v186, 0\n"
    "ds_read_b64_tr_b16 a[28:29], v13 offset:32768\n"
    "ds_read_b64_tr_b16 a[30:31], v13 offset:34816\n"
    "v_pk_max_i16 v187, v187, 0\n"
    "s_add_u32 s49, s30, 147456\n"
    "s_add_u32 s49, s49, s64\n"
    "buffer_store_dwordx4 v[184:187], v14, s[16:19], s49 offen sc1\n"
    "v_pk_fma_f32 v[192:193], v[84:85], v[96:97], v[192:193] op_sel_hi:[1,0,1]\n"
    "v_pk_fma_f32 v[194:195], v[86:87], v[96:97], v[194:195] op_sel_hi:[1,0,1]\n"
    "v_pk_fma_f32 v[196:197], v[84:85], v[98:99], v[196:197] op_sel_hi:[1,0,1]\n"
    "v_pk_fma_f32 v[198:199], v[86:87], v[98:99], v[198:199] op_sel_hi:[1,0,1]\n"
    "v_pk_fma_f32 v[192:193], v[192:193], v[96:97], v[56:57] op_sel:[0,1,0]\n"
    "v_pk_fma_f32 v[194:195], v[194:195], v[96:97], v[58:59] op_sel:[0,1,0]\n"
    "v_pk_fma_f32 v[196:197], v[196:197], v[98:99], v[56:57] op_sel:[0,1,0]\n"
    "v_pk_fma_f32 v[198:199], v[198:199], v[98:99], v[58:59] op_sel:[0,1,0]\n"
    "v_mul_f32_e64 v192, v192, |v192|\n"
    "v_mul_f32_e64 v193, v193, |v193|\n"
    "v_mul_f32_e64 v194, v194, |v194|\n"
    "s_mov_b32 s50, 163840\n"
    "s_add_u32 m0, s59, 16384\n"
    "v_mul_f32_e64 v195, v195, |v195|\n"
    "v_mul_f32_e64 v196, v196, |v196|\n"
    "v_mul_f32_e64 v197, v197, |v197|\n"
    "v_mul_f32_e64 v198, v198, |v198|\n"
    "v_mul_f32_e64 v199, v199, |v199|\n"
    "v_cvt_pk_bf16_f32 v192, v192, v193\n"
    "v_cvt_pk_bf16_f32 v193, v194, v195\n"
    "v_cvt_pk_bf16_f32 v194, v196, v197\n"
    "v_cvt_pk_bf16_f32 v195, v198, v199\n"
    "v_pk_max_i16 v192, v192, 0\n"
    "v_pk_max_i16 v193, v193, 0\n"
    "v_pk_max_i16 v194, v194, 0\n"
    "v_pk_max_i16 v195, v195, 0\n"
    "s_add_u32 s49, s27, 0\n"
    "s_add_u32 s49, s49, s64\n"
    "buffer_load_dwordx4 v2, s[12:15], s50 offen offset:0 lds\n"
    "buffer_store_dwordx4 v[192:195], v14, s[16:19], s49 offen sc1\n"
    "v_pk_fma_f32 v[200:201], v[84:85], v[100:101], v[200:201] op_sel_hi:[1,0,1]\n"
    "v_pk_fma_f32 v[202:203], v[86:87], v[100:101], v[202:203] op_sel_hi:[1,0,1]\n"
    "v_pk_fma_f32 v[204:205], v[84:85], v[102:103], v[204:205] op_sel_hi:[1,0,1]\n"
    "v_pk_fma_f32 v[206:207], v[86:87], v[102:103], v[206:207] op_sel_hi:[1,0,1]\n"
    "v_pk_fma_f32 v[200:201], v[200:201], v[100:101], v[56:57] op_sel:[0,1,0]\n"
    "v_pk_fma_f32 v[202:203], v[202:203], v[100:101], v[58:59] op_sel:[0,1,0]\n"
    "v_pk_fma_f32 v[204:205], v[204:205], v[102:103], v[56:57] op_sel:[0,1,0]\n"
    "v_pk_fma_f32 v[206:207], v[206:207], v[102:103], v[58:59] op_sel:[0,1,0]\n"
    "v_mul_f32_e64 v200, v200, |v200|\n"
    "v_mul_f32_e64 v201, v201, |v201|\n"
    "v_mul_f32_e64 v202, v202, |v202|\n"
    "v_mul_f32_e64 v203, v203, |v203|\n"
    "v_mul_f32_e64 v204, v204, |v204|\n"
    "v_mul_f32_e64 v205, v205, |v205|\n"
    "v_mul_f32_e64 v206, v206, |v206|\n"
    "buffer_load_dwordx4 v3, s[12:15], s50 offen offset:1024 lds\n"
    "v_mul_f32_e64 v207, v207, |v207|\n"
    "v_cvt_pk_bf16_f32 v200, v200, v201\n"
    "v_cvt_pk_bf16_f32 v201, v202, v203\n"
    "v_cvt_pk_bf16_f32 v202, v204, v205\n"
    "v_cvt_pk_bf16_f32 v203, v206, v207\n"
    "v_pk_max_i16 v200, v200, 0\n"
    "v_pk_max_i16 v201, v201, 0\n"
    "v_pk_max_i16 v202, v202, 0\n"
    "v_pk_max_i16 v203, v203, 0\n"
    "s_add_u32 s49, s27, 147456\n"
    "s_add_u32 s49, s49, s64\n"
    "buffer_store_dwordx4 v[200:203], v14, s[16:19], s49 offen sc1\n"
    "v_pk_fma_f32 v[208:209], v[88:89], v[96:97], v[208:209] op_sel_hi:[1,0,1]\n"
    "v_pk_fma_f32 v[210:211], v[90:91], v[96:97], v[210:211] op_sel_hi:[1,0,1]\n"
    "v_pk_fma_f32 v[212:213], v[88:89], v[98:99], v[212:213] op_sel_hi:[1,0,1]\n"
    "buffer_load_dwordx4 v4, s[12:15], s50 offen offset:2048 lds\n"
    "v_pk_fma_f32 v[214:215], v[90:91], v[98:99], v[214:215] op_sel_hi:[1,0,1]\n"
    "v_pk_fma_f32 v[208:209], v[208:209], v[96:97], v[60:61] op_sel:[0,1,0]\n"
    "v_pk_fma_f32 v[210:211], v[210:211], v[96:97], v[62:63] op_sel:[0,1,0]\n"
    "v_pk_fma_f32 v[212:213], v[212:213], v[98:99], v[60:61] op_sel:[0,1,0]\n"
    "v_pk_fma_f32 v[214:215], v[214:215], v[98:99], v[62:63] op_sel:[0,1,0]\n"
    "v_mul_f32_e64 v208, v208, |v208|\n"
    "v_mul_f32_e64 v209, v209, |v209|\n"
    "v_mul_f32_e64 v210, v210, |v210|\n"
    "v_mul_f32_e64 v211, v211, |v211|\n"
    "v_mul_f32_e64 v212, v212, |v212|\n"
    "v_mul_f32_e64 v213, v213, |v213|\n"
    "v_mul_f32_e64 v214, v214, |v214|\n"
    "v_mul_f32_e64 v215, v215, |v215|\n"
    "v_cvt_pk_bf16_f32 v208, v208, v209\n"
    "v_cvt_pk_bf16_f32 v209, v210, v211\n"
    "buffer_load_dwordx4 v5, s[12:15], s50 offen offset:3072 lds\n"
    "v_cvt_pk_bf16_f32 v210, v212, v213\n"
    "v_cvt_pk_bf16_f32 v211, v214, v215\n"
    "v_pk_max_i16 v208, v208, 0\n"
    "v_pk_max_i16 v209, v209, 0\n"
    "v_pk_max_i16 v210, v210, 0\n"
    "v_pk_max_i16 v211, v211, 0\n"
    "s_add_u32 s49, s27, 1024\n"
    "s_add_u32 s49, s49, s64\n"
    "buffer_store_dwordx4 v[208:211], v14, s[16:19], s49 offen sc1\n"
    "v_pk_fma_f32 v[216:217], v[88:89], v[100:101], v[216:217] op_sel_hi:[1,0,1]\n"
    "v_pk_fma_f32 v[218:219], v[90:91], v[100:101], v[218:219] op_sel_hi:[1,0,1]\n"
    "v_pk_fma_f32 v[220:221], v[88:89], v[102:103], v[220:221] op_sel_hi:[1,0,1]\n"
    "v_pk_fma_f32 v[222:223], v[90:91], v[102:103], v[222:223] op_sel_hi:[1,0,1]\n"
    "v_pk_fma_f32 v[216:217], v[216:217], v[100:101], v[60:61] op_sel:[0,1,0]\n"
    "v_pk_fma_f32 v[218:219], v[218:219], v[100:101], v[62:63] op_sel:[0,1,0]\n"
    "s_waitcnt vmcnt(23)\n"
    "v_pk_fma_f32 v[220:221], v[220:221], v[102:103], v[60:61] op_sel:[0,1,0]\n"
    "v_pk_fma_f32 v[222:223], v[222:223], v[102:103], v[62:63] op_sel:[0,1,0]\n"
    "v_mul_f32_e64 v216, v216, |v216|\n"
    "v_mul_f32_e64 v217, v217, |v217|\n"
    "v_mul_f32_e64 v218, v218, |v218|\n"
    "v_mul_f32_e64 v219, v219, |v219|\n"
    "v_mul_f32_e64 v220, v220, |v220|\n"
    "v_mul_f32_e64 v221, v221, |v221|\n"
    "v_mul_f32_e64 v222, v222, |v222|\n"
    "v_mul_f32_e64 v223, v223, |v223|\n"
    "v_cvt_pk_bf16_f32 v216, v216, v217\n"
    "v_cvt_pk_bf16_f32 v217, v218, v219\n"
    "v_cvt_pk_bf16_f32 v218, v220, v221\n"
    "v_cvt_pk_bf16_f32 v219, v222, v223\n"
    "v_pk_max_i16 v216, v216, 0\n"
    "ds_read_b64_tr_b16 a[32:33], v10 offset:49152\n"
    "ds_read_b64_tr_b16 a[34:35], v10 offset:51200\n"
    "v_pk_max_i16 v217, v217, 0\n"
    "v_pk_max_i16 v218, v218, 0\n"
    "v_pk_max_i16 v219, v219, 0\n"
    "s_add_u32 s49, s27, 148480\n"
    "s_add_u32 s49, s49, s64\n"
    "buffer_store_dwordx4 v[216:219], v14, s[16:19], s49 offen sc1\n"
    "v_pk_fma_f32 v[224:225], v[92:93], v[96:97], v[224:225] op_sel_hi:[1,0,1]\n"
    "v_pk_fma_f32 v[226:227], v[94:95], v[96:97], v[226:227] op_sel_hi:[1,0,1]\n"
    "v_pk_fma_f32 v[228:229], v[92:93], v[98:99], v[228:229] op_sel_hi:[1,0,1]\n"
    "v_pk_fma_f32 v[230:231], v[94:95], v[98:99], v[230:231] op_sel_hi:[1,0,1]\n"
    "v_pk_fma_f32 v[224:225], v[224:225], v[96:97], v[64:65] op_sel:[0,1,0]\n"
    "v_pk_fma_f32 v[226:227], v[226:227], v[96:97], v[66:67] op_sel:[0,1,0]\n"
    "v_pk_fma_f32 v[228:229], v[228:229], v[98:99], v[64:65] op_sel:[0,1,0]\n"
    "v_pk_fma_f32 v[230:231], v[230:231], v[98:99], v[66:67] op_sel:[0,1,0]\n"
    "v_mul_f32_e64 v224, v224, |v224|\n"
    "ds_read_b64_tr_b16 a[36:37], v11 offset:49152\n"
    "ds_read_b64_tr_b16 a[38:39], v11 offset:51200\n"
    "v_mul_f32_e64 v225, v225, |v225|\n"
    "v_mul_f32_e64 v226, v226, |v226|\n"
    "v_mul_f32_e64 v227, v227, |v227|\n"
    "v_mul_f32_e64 v228, v228, |v228|\n"
    "v_mul_f32_e64 v229, v229, |v229|\n"
    "v_mul_f32_e64 v230, v230, |v230|\n"
    "v_mul_f32_e64 v231, v231, |v231|\n"
    "v_cvt_pk_bf16_f32 v224, v224, v225\n"
    "v_cvt_pk_bf16_f32 v225, v226, v227\n"
    "v_cvt_pk_bf16_f32 v226, v228, v229\n"
    "v_cvt_pk_bf16_f32 v227, v230, v231\n"
    "v_pk_max_i16 v224, v224, 0\n"
    "v_pk_max_i16 v225, v225, 0\n"
    "v_pk_max_i16 v226, v226, 0\n"
    "v_pk_max_i16 v227, v227, 0\n"
    "ds_read_b64_tr_b16 a[40:41], v12 offset:49152\n"
    "ds_read_b64_tr_b16 a[42:43], v12 offset:51200\n"
    "s_add_u32 s49, s27, 2048\n"
    "s_add_u32 s49, s49, s64\n"
    "buffer_store_dwordx4 v[224:227], v14, s[16:19], s49 offen sc1\n"
    "v_pk_fma_f32 v[232:233], v[92:93], v[100:101], v[232:233] op_sel_hi:[1,0,1]\n"
    "v_pk_fma_f32 v[234:235], v[94:95], v[100:101], v[234:235] op_sel_hi:[1,0,1]\n"
    "v_pk_fma_f32 v[236:237], v[92:93], v[102:103], v[236:237] op_sel_hi:[1,0,1]\n"
    "v_pk_fma_f32 v[238:239], v[94:95], v[102:103], v[238:239] op_sel_hi:[1,0,1]\n"
    "v_pk_fma_f32 v[232:233], v[232:233], v[100:101], v[64:65] op_sel:[0,1,0]\n"
    "v_pk_fma_f32 v[234:235], v[234:235], v[100:101], v[66:67] op_sel:[0,1,0]\n"
    "v_pk_fma_f32 v[236:237], v[236:237], v[102:103], v[64:65] op_sel:[0,1,0]\n"
    "v_pk_fma_f32 v[238:239], v[238:239], v[102:103], v[66:67] op_sel:[0,1,0]\n"
    "v_mul_f32_e64 v232, v232, |v232|\n"
    "v_mul_f32_e64 v233, v233, |v233|\n"
    "v_mul_f32_e64 v234, v234, |v234|\n"
    "v_mul_f32_e64 v235, v235, |v235|\n"
    "ds_read_b64_tr_b16 a[44:45], v13 offset:49152\n"
    "ds_read_b64_tr_b16 a[46:47], v13 offset:51200\n"
    "v_mul_f32_e64 v236, v236, |v236|\n"
    "v_mul_f32_e64 v237, v237, |v237|\n"
    "v_mul_f32_e64 v238, v238, |v238|\n"
    "v_mul_f32_e64 v239, v239, |v239|\n"
    "v_cvt_pk_bf16_f32 v232, v232, v233\n"
    "v_cvt_pk_bf16_f32 v233, v234, v235\n"
    "v_cvt_pk_bf16_f32 v234, v236, v237\n"
    "v_cvt_pk_bf16_f32 v235, v238, v239\n"
    "v_pk_max_i16 v232, v232, 0\n"
    "v_pk_max_i16 v233, v233, 0\n"
    "v_pk_max_i16 v234, v234, 0\n"
    "v_pk_max_i16 v235, v235, 0\n"
    "s_add_u32 s49, s27, 149504\n"
    "s_add_u32 s49, s49, s64\n"
    "buffer_store_dwordx4 v[232:235], v14, s[16:19], s49 offen sc1\n"
    "s_mov_b32 s50, 196608\n"
    "s_add_u32 m0, s59, 32768\n"
    "s_nop 0\n"
    "buffer_load_dwordx4 v2, s[12:15], s50 offen offset:0 lds\n"
    "buffer_load_dwordx4 v3, s[12:15], s50 offen offset:1024 lds\n"
    "buffer_load_dwordx4 v4, s[12:15], s50 offen offset:2048 lds\n"
    "buffer_load_dwordx4 v5, s[12:15], s50 offen offset:3072 lds\n"
    "s_waitcnt vmcnt(26)\n"
    "ds_read_b64_tr_b16 a[48:49], v6 offset:0\n"
    "ds_read_b64_tr_b16 a[50:51], v6 offset:2048\n"
    "ds_read_b64_tr_b16 a[52:53], v7 offset:0\n"
    "ds_read_b64_tr_b16 a[54:55], v7 offset:2048\n"
    "ds_read_b64_tr_b16 a[56:57], v8 offset:0\n"
    "ds_read_b64_tr_b16 a[58:59], v8 offset:2048\n"
    "ds_read_b64_tr_b16 a[60:61], v9 offset:0\n"
    "ds_read_b64_tr_b16 a[62:63], v9 offset:2048\n"
    "s_waitcnt lgkmcnt(15)\n"
    "s_mov_b32 s50, 229376\n"
    "s_add_u32 m0, s59, 114688\n"
    "s_nop 0\n"
    "buffer_load_dwordx4 v2, s[12:15], s50 offen offset:0 lds\n"
    "buffer_load_dwordx4 v3, s[12:15], s50 offen offset:1024 lds\n"
    "buffer_load_dwordx4 v4, s[12:15], s50 offen offset:2048 lds\n"
    "buffer_load_dwordx4 v5, s[12:15], s50 offen offset:3072 lds\n"
    "v_mov_b32 v40, s66\n"
    "v_mov_b32 v41, s67\n"
    "s_waitcnt vmcnt(8)\n"
    "s_barrier\n"
    "buffer_store_dwordx2 v[40:41], v15, s[16:19], 0 offen sc1\n"
    "L_join:\n"
    "s_lshr_b32 s48, s3, 1\n"
    "s_xor_b32 s48, s48, 1\n"
    "s_lshl_b32 s48, s48, 15\n"
    "s_add_u32 s48, s48, s59\n"
    "v_and_b32 v104, 15, v1\n"
    "v_lshrrev_b32 v105, 4, v1\n"
    "v_and_b32 v106, 7, v1\n"
    "v_xor_b32 v105, v105, v106\n"
    "v_lshlrev_b32 v105, 4, v105\n"
    "v_lshl_add_u32 v105, v104, 8, v105\n"
    "v_add_u32 v17, s48, v105\n"
    "v_xor_b32 v18, 64, v17\n"
    "v_xor_b32 v19, 0x8000, v17\n"
    "v_xor_b32 v20, 0x8000, v18\n"
    "v_lshrrev_b32 v104, 3, v1\n"
    "v_lshlrev_b32 v105, 1, v106\n"
    "v_xor_b32 v105, v105, v104\n"
    "v_lshlrev_b32 v105, 4, v105\n"
    "v_lshl_add_u32 v105, v104, 8, v105\n"
    "s_lshl_b32 s48, s3, 14\n"
    "v_add_u32 v21, s48, v105\n"
    "v_xor_b32 v22, 16, v21\n"
    "s_lshr_b32 s48, s31, 2\n"
    "s_lshl_b32 s48, s48, 18\n"
    "s_bfe_u32 s49, s31, 0x10001\n"
    "s_lshl_b32 s49, s49, 10\n"
    "s_add_u32 s48, s48, s49\n"
    "s_and_b32 s49, s31, 1\n"
    "s_lshl_b32 s49, s49, 17\n"
    "s_add_u32 s48, s48, s49\n"
    "s_lshl_b32 s49, s3, 15\n"
    "s_add_u32 s48, s48, s49\n"
    "s_bfe_u32 s49, s2, 0x30003\n"
    "s_lshl_b32 s49, s49, 7\n"
    "v_lshl_add_u32 v25, v106, 4, s49\n"
    "v_lshl_add_u32 v23, v104, 11, v25\n"
    "v_add_u32 v23, s48, v23\n"
    "v_add_u32 v24, 16384, v23\n"
    "s_mov_b64 s[20:21], s[34:35]\n"
    "s_mov_b32 s22, 1024\n"
    "s_mov_b32 s26, 2097152\n"
    "s_mov_b32 s27, 0x20000\n"
    "s_waitcnt vmcnt(17)\n"
    "ds_read_b64_tr_b16 a[64:65], v6 offset:49152\n"
    "ds_read_b64_tr_b16 a[66:67], v6 offset:51200\n"
    "ds_read_b64_tr_b16 a[68:69], v7 offset:49152\n"
    "ds_read_b64_tr_b16 a[70:71], v7 offset:51200\n"
    "ds_read_b64_tr_b16 a[72:73], v8 offset:49152\n"
    "ds_read_b64_tr_b16 a[74:75], v8 offset:51200\n"
    "ds_read_b64_tr_b16 a[76:77], v9 offset:49152\n"
    "ds_read_b64_tr_b16 a[78:79], v9 offset:51200\n"
    "s_waitcnt lgkmcnt(15)\n"
    "s_mov_b32 s50, 262144\n"
    "s_add_u32 m0, s59, 131072\n"
    "s_nop 0\n"
    "buffer_load_dwordx4 v2, s[12:15], s50 offen offset:0 lds\n"
    "buffer_load_dwordx4 v3, s[12:15], s50 offen offset:1024 lds\n"
    "buffer_load_dwordx4 v4, s[12:15], s50 offen offset:2048 lds\n"
    "buffer_load_dwordx4 v5, s[12:15], s50 offen offset:3072 lds\n"
    "s_waitcnt vmcnt(17)\n"
    "ds_read_b64_tr_b16 a[80:81], v6 offset:16384\n"
    "ds_read_b64_tr_b16 a[82:83], v6 offset:18432\n"
    "ds_read_b64_tr_b16 a[84:85], v7 offset:16384\n"
    "ds_read_b64_tr_b16 a[86:87], v7 offset:18432\n"
    "ds_read_b64_tr_b16 a[88:89], v8 offset:16384\n"
    "ds_read_b64_tr_b16 a[90:91], v8 offset:18432\n"
    "ds_read_b64_tr_b16 a[92:93], v9 offset:16384\n"
    "ds_read_b64_tr_b16 a[94:95], v9 offset:18432\n"
    "s_waitcnt lgkmcnt(15)\n"
    "s_mov_b32 s50, 294912\n"
    "s_add_u32 m0, s59, 147456\n"
    "s_nop 0\n"
    "buffer_load_dwordx4 v2, s[12:15], s50 offen offset:0 lds\n"
    "buffer_load_dwordx4 v3, s[12:15], s50 offen offset:1024 lds\n"
    "buffer_load_dwordx4 v4, s[12:15], s50 offen offset:2048 lds\n"
    "buffer_load_dwordx4 v5, s[12:15], s50 offen offset:3072 lds\n"
    "s_waitcnt vmcnt(13)\n"
    "ds_read_b64_tr_b16 a[96:97], v6 offset:32768\n"
    "ds_read_b64_tr_b16 a[98:99], v6 offset:34816\n"
    "ds_read_b64_tr_b16 a[100:101], v7 offset:32768\n"
    "ds_read_b64_tr_b16 a[102:103], v7 offset:34816\n"
    "ds_read_b64_tr_b16 a[104:105], v8 offset:32768\n"
    "ds_read_b64_tr_b16 a[106:107], v8 offset:34816\n"
    "ds_read_b64_tr_b16 a[108:109], v9 offset:32768\n"
    "ds_read_b64_tr_b16 a[110:111], v9 offset:34816\n"
    "s_waitcnt lgkmcnt(15)\n"
    "s_mov_b32 s50, 327680\n"
    "s_add_u32 m0, s59, 0\n"
    "s_nop 0\n"
    "buffer_load_dwordx4 v2, s[12:15], s50 offen offset:0 lds\n"
    "buffer_load_dwordx4 v3, s[12:15], s50 offen offset:1024 lds\n"
    "buffer_load_dwordx4 v4, s[12:15], s50 offen offset:2048 lds\n"
    "buffer_load_dwordx4 v5, s[12:15], s50 offen offset:3072 lds\n"
    "s_waitcnt vmcnt(13)\n"
    "ds_read_b64_tr_b16 a[112:113], v10 offset:16384\n"
    "ds_read_b64_tr_b16 a[114:115], v10 offset:18432\n"
    "ds_read_b64_tr_b16 a[116:117], v11 offset:16384\n"
    "ds_read_b64_tr_b16 a[118:119], v11 offset:18432\n"
    "ds_read_b64_tr_b16 a[120:121], v12 offset:16384\n"
    "ds_read_b64_tr_b16 a[122:123], v12 offset:18432\n"
    "ds_read_b64_tr_b16 a[124:125], v13 offset:16384\n"
    "ds_read_b64_tr_b16 a[126:127], v13 offset:18432\n"
    "s_waitcnt lgkmcnt(15)\n"
    "s_mov_b32 s50, 360448\n"
    "s_add_u32 m0, s59, 49152\n"
    "s_nop 0\n"
    "buffer_load_dwordx4 v2, s[12:15], s50 offen offset:0 lds\n"
    "buffer_load_dwordx4 v3, s[12:15], s50 offen offset:1024 lds\n"
    "buffer_load_dwordx4 v4, s[12:15], s50 offen offset:2048 lds\n"
    "buffer_load_dwordx4 v5, s[12:15], s50 offen offset:3072 lds\n"
    "s_waitcnt vmcnt(12)\n"
    "ds_read_b64_tr_b16 a[128:129], v10 offset:32768\n"
    "ds_read_b64_tr_b16 a[130:131], v10 offset:34816\n"
    "ds_read_b64_tr_b16 a[132:133], v11 offset:32768\n"
    "ds_read_b64_tr_b16 a[134:135], v11 offset:34816\n"
    "ds_read_b64_tr_b16 a[136:137], v12 offset:32768\n"
    "ds_read_b64_tr_b16 a[138:139], v12 offset:34816\n"
    "ds_read_b64_tr_b16 a[140:141], v13 offset:32768\n"
    "ds_read_b64_tr_b16 a[142:143], v13 offset:34816\n"
    "s_waitcnt lgkmcnt(15)\n"
    "s_mov_b32 s50, 393216\n"
    "s_add_u32 m0, s59, 16384\n"
    "s_nop 0\n"
    "buffer_load_dwordx4 v2, s[12:15], s50 offen offset:0 lds\n"
    "buffer_load_dwordx4 v3, s[12:15], s50 offen offset:1024 lds\n"
    "buffer_load_dwordx4 v4, s[12:15], s50 offen offset:2048 lds\n"
    "buffer_load_dwordx4 v5, s[12:15], s50 offen offset:3072 lds\n"
    "s_waitcnt vmcnt(12)\n"
    "ds_read_b64_tr_b16 a[144:145], v10 offset:49152\n"
    "ds_read_b64_tr_b16 a[146:147], v10 offset:51200\n"
    "ds_read_b64_tr_b16 a[148:149], v11 offset:49152\n"
    "ds_read_b64_tr_b16 a[150:151], v11 offset:51200\n"
    "ds_read_b64_tr_b16 a[152:153], v12 offset:49152\n"
    "ds_read_b64_tr_b16 a[154:155], v12 offset:51200\n"
    "ds_read_b64_tr_b16 a[156:157], v13 offset:49152\n"
    "ds_read_b64_tr_b16 a[158:159], v13 offset:51200\n"
    "s_waitcnt lgkmcnt(15)\n"
    "s_mov_b32 s50, 425984\n"
    "s_add_u32 m0, s59, 32768\n"
    "s_nop 0\n"
    "buffer_load_dwordx4 v2, s[12:15], s50 offen offset:0 lds\n"
    "buffer_load_dwordx4 v3, s[12:15], s50 offen offset:1024 lds\n"
    "buffer_load_dwordx4 v4, s[12:15], s50 offen offset:2048 lds\n"
    "buffer_load_dwordx4 v5, s[12:15], s50 offen offset:3072 lds\n"
    "s_waitcnt vmcnt(12)\n"
    "ds_read_b64_tr_b16 a[160:161], v6 offset:0\n"
    "ds_read_b64_tr_b16 a[162:163], v6 offset:2048\n"
    "ds_read_b64_tr_b16 a[164:165], v7 offset:0\n"
    "ds_read_b64_tr_b16 a[166:167], v7 offset:2048\n"
    "ds_read_b64_tr_b16 a[168:169], v8 offset:0\n"
    "ds_read_b64_tr_b16 a[170:171], v8 offset:2048\n"
    "ds_read_b64_tr_b16 a[172:173], v9 offset:0\n"
    "ds_read_b64_tr_b16 a[174:175], v9 offset:2048\n"
    "s_waitcnt lgkmcnt(15)\n"
    "s_mov_b32 s50, 458752\n"
    "s_add_u32 m0, s59, 114688\n"
    "s_nop 0\n"
    "buffer_load_dwordx4 v2, s[12:15], s50 offen offset:0 lds\n"
    "buffer_load_dwordx4 v3, s[12:15], s50 offen offset:1024 lds\n"
    "buffer_load_dwordx4 v4, s[12:15], s50 offen offset:2048 lds\n"
    "buffer_load_dwordx4 v5, s[12:15], s50 offen offset:3072 lds\n"
    "s_waitcnt vmcnt(12)\n"
    "ds_read_b64_tr_b16 a[176:177], v6 offset:49152\n"
    "ds_read_b64_tr_b16 a[178:179], v6 offset:51200\n"
    "ds_read_b64_tr_b16 a[180:181], v7 offset:49152\n"
    "ds_read_b64_tr_b16 a[182:183], v7 offset:51200\n"
    "ds_read_b64_tr_b16 a[184:185], v8 offset:49152\n"
    "ds_read_b64_tr_b16 a[186:187], v8 offset:51200\n"
    "ds_read_b64_tr_b16 a[188:189], v9 offset:49152\n"
    "ds_read_b64_tr_b16 a[190:191], v9 offset:51200\n"
    "s_waitcnt lgkmcnt(15)\n"
    "s_mov_b32 s50, 491520\n"
    "s_add_u32 m0, s59, 131072\n"
    "s_nop 0\n"
    "buffer_load_dwordx4 v2, s[12:15], s50 offen offset:0 lds\n"
    "buffer_load_dwordx4 v3, s[12:15], s50 offen offset:1024 lds\n"
    "buffer_load_dwordx4 v4, s[12:15], s50 offen offset:2048 lds\n"
    "buffer_load_dwordx4 v5, s[12:15], s50 offen offset:3072 lds\n"
    "s_waitcnt vmcnt(12)\n"
    "ds_read_b64_tr_b16 a[192:193], v6 offset:16384\n"
    "ds_read_b64_tr_b16 a[194:195], v6 offset:18432\n"
    "ds_read_b64_tr_b16 a[196:197], v7 offset:16384\n"
    "ds_read_b64_tr_b16 a[198:199], v7 offset:18432\n"
    "ds_read_b64_tr_b16 a[200:201], v8 offset:16384\n"
    "ds_read_b64_tr_b16 a[202:203], v8 offset:18432\n"
    "ds_read_b64_tr_b16 a[204:205], v9 offset:16384\n"
    "ds_read_b64_tr_b16 a[206:207], v9 offset:18432\n"
    "buffer_load_dwordx2 v[104:105], v16, s[16:19], 0 offen sc1\n"
    "s_waitcnt lgkmcnt(15)\n"
    "s_mov_b32 s50, 524288\n"
    "s_add_u32 m0, s59, 147456\n"
    "s_nop 0\n"
    "buffer_load_dwordx4 v2, s[12:15], s50 offen offset:0 lds\n"
    "buffer_load_dwordx4 v3, s[12:15], s50 offen offset:1024 lds\n"
    "buffer_load_dwordx4 v4, s[12:15], s50 offen offset:2048 lds\n"
    "buffer_load_dwordx4 v5, s[12:15], s50 offen offset:3072 lds\n"
    "s_waitcnt vmcnt(13)\n"
    "ds_read_b64_tr_b16 a[208:209], v6 offset:32768\n"
    "ds_read_b64_tr_b16 a[210:211], v6 offset:34816\n"
    "ds_read_b64_tr_b16 a[212:213], v7 offset:32768\n"
    "ds_read_b64_tr_b16 a[214:215], v7 offset:34816\n"
    "ds_read_b64_tr_b16 a[216:217], v8 offset:32768\n"
    "ds_read_b64_tr_b16 a[218:219], v8 offset:34816\n"
    "ds_read_b64_tr_b16 a[220:221], v9 offset:32768\n"
    "ds_read_b64_tr_b16 a[222:223], v9 offset:34816\n"
    "s_waitcnt lgkmcnt(15)\n"
    "s_mov_b32 s50, 557056\n"
    "s_add_u32 m0, s59, 0\n"
    "s_nop 0\n"
    "buffer_load_dwordx4 v2, s[12:15], s50 offen offset:0 lds\n"
    "buffer_load_dwordx4 v3, s[12:15], s50 offen offset:1024 lds\n"
    "buffer_load_dwordx4 v4, s[12:15], s50 offen offset:2048 lds\n"
    "buffer_load_dwordx4 v5, s[12:15], s50 offen offset:3072 lds\n"
    "s_waitcnt vmcnt(13)\n"
    "ds_read_b64_tr_b16 a[224:225], v10 offset:16384\n"
    "ds_read_b64_tr_b16 a[226:227], v10 offset:18432\n"
    "ds_read_b64_tr_b16 a[228:229], v11 offset:16384\n"
    "ds_read_b64_tr_b16 a[230:231], v11 offset:18432\n"
    "ds_read_b64_tr_b16 a[232:233], v12 offset:16384\n"
    "ds_read_b64_tr_b16 a[234:235], v12 offset:18432\n"
    "ds_read_b64_tr_b16 a[236:237], v13 offset:16384\n"
    "ds_read_b64_tr_b16 a[238:239], v13 offset:18432\n"
    "s_waitcnt lgkmcnt(15)\n"
    "s_mov_b32 s50, 589824\n"
    "s_add_u32 m0, s59, 49152\n"
    "s_nop 0\n"
    "buffer_load_dwordx4 v2, s[12:15], s50 offen offset:0 lds\n"
    "buffer_load_dwordx4 v3, s[12:15], s50 offen offset:1024 lds\n"
    "buffer_load_dwordx4 v4, s[12:15], s50 offen offset:2048 lds\n"
    "buffer_load_dwordx4 v5, s[12:15], s50 offen offset:3072 lds\n"
    "s_waitcnt vmcnt(12)\n"
    "v_cmp_eq_u64 vcc, s[66:67], v[104:105]\n"
    "s_nop 0\n"
    "s_andn2_b32 s48, s65, vcc_lo\n"
    "s_cbranch_scc1 L_pre\n"
    "L_pok:\n"
    "s_mov_b32 s49, s62\n"
    "s_add_u32 s62, s62, 2048\n"
    "buffer_load_dwordx4 v[40:43], v14, s[40:43], s49 offen offset:0 sc1\n"
    "buffer_load_dwordx4 v[44:47], v14, s[40:43], s49 offen offset:1024 sc1\n"
    "s_mov_b32 s49, s62\n"
    "s_add_u32 s62, s62, 2048\n"
    "buffer_load_dwordx4 v[48:51], v14, s[40:43], s49 offen offset:0 sc1\n"
    "buffer_load_dwordx4 v[52:55], v14, s[40:43], s49 offen offset:1024 sc1\n"
    "s_mov_b32 s49, s62\n"
    "s_add_u32 s62, s62, 2048\n"
    "buffer_load_dwordx4 v[56:59], v14, s[40:43], s49 offen offset:0 sc1\n"
    "buffer_load_dwordx4 v[60:63], v14, s[40:43], s49 offen offset:1024 sc1\n"
    "s_mov_b32 s49, s62\n"
    "s_add_u32 s62, s62, 2048\n"
    "buffer_load_dwordx4 v[64:67], v14, s[40:43], s49 offen offset:0 sc1\n"
    "buffer_load_dwordx4 v[68:71], v14, s[40:43], s49 offen offset:1024 sc1\n"
    "s_mov_b32 s49, s62\n"
    "s_add_u32 s62, s62, 2048\n"
    "buffer_load_dwordx4 v[72:75], v14, s[40:43], s49 offen offset:0 sc1\n"
    "buffer_load_dwordx4 v[76:79], v14, s[40:43], s49 offen offset:1024 sc1\n"
    "s_mov_b32 s49, s62\n"
    "s_add_u32 s62, s62, 2048\n"
    "buffer_load_dwordx4 v[80:83], v14, s[40:43], s49 offen offset:0 sc1\n"
    "buffer_load_dwordx4 v[84:87], v14, s[40:43], s49 offen offset:1024 sc1\n"
    "s_mov_b32 s49, s62\n"
    "s_add_u32 s62, s62, 2048\n"
    "buffer_load_dwordx4 v[88:91], v14, s[40:43], s49 offen offset:0 sc1\n"
    "buffer_load_dwordx4 v[92:95], v14, s[40:43], s49 offen offset:1024 sc1\n"
    "s_waitcnt vmcnt(27)\n"
    "ds_read_b64_tr_b16 a[240:241], v10 offset:32768\n"
    "ds_read_b64_tr_b16 a[242:243], v10 offset:34816\n"
    "ds_read_b64_tr_b16 a[244:245], v11 offset:32768\n"
    "ds_read_b64_tr_b16 a[246:247], v11 offset:34816\n"
    "ds_read_b64_tr_b16 a[248:249], v12 offset:32768\n"
    "ds_read_b64_tr_b16 a[250:251], v12 offset:34816\n"
    "ds_read_b64_tr_b16 a[252:253], v13 offset:32768\n"
    "ds_read_b64_tr_b16 a[254:255], v13 offset:34816\n"
    "s_waitcnt vmcnt(22)\n"
    "ds_read_b64_tr_b16 v[192:193], v10 offset:49152\n"
    "ds_read_b64_tr_b16 v[194:195], v10 offset:51200\n"
    "ds_read_b64_tr_b16 v[196:197], v11 offset:49152\n"
    "ds_read_b64_tr_b16 v[198:199], v11 offset:51200\n"
    "ds_read_b64_tr_b16 v[200:201], v12 offset:49152\n"
    "ds_read_b64_tr_b16 v[202:203], v12 offset:51200\n"
    "ds_read_b64_tr_b16 v[204:205], v13 offset:49152\n"
    "ds_read_b64_tr_b16 v[206:207], v13 offset:51200\n"
    "s_waitcnt vmcnt(18)\n"
    "ds_read_b64_tr_b16 v[208:209], v6 offset:0\n"
    "ds_read_b64_tr_b16 v[210:211], v6 offset:2048\n"
    "ds_read_b64_tr_b16 v[212:213], v7 offset:0\n"
    "ds_read_b64_tr_b16 v[214:215], v7 offset:2048\n"
    "ds_read_b64_tr_b16 v[216:217], v8 offset:0\n"
    "ds_read_b64_tr_b16 v[218:219], v8 offset:2048\n"
    "ds_read_b64_tr_b16 v[220:221], v9 offset:0\n"
    "ds_read_b64_tr_b16 v[222:223], v9 offset:2048\n"
    "s_waitcnt vmcnt(14)\n"
    "ds_read_b64_tr_b16 v[224:225], v6 offset:49152\n"
    "ds_read_b64_tr_b16 v[226:227], v6 offset:51200\n"
    "ds_read_b64_tr_b16 v[228:229], v7 offset:49152\n"
    "ds_read_b64_tr_b16 v[230:231], v7 offset:51200\n"
    "ds_read_b64_tr_b16 v[232:233], v8 offset:49152\n"
    "ds_read_b64_tr_b16 v[234:235], v8 offset:51200\n"
    "ds_read_b64_tr_b16 v[236:237], v9 offset:49152\n"
    "ds_read_b64_tr_b16 v[238:239], v9 offset:51200\n"
    "s_waitcnt vmcnt(12)\n"
    "v_swap_b32 v42, v44\n"
    "v_swap_b32 v43, v45\n"
    "s_waitcnt lgkmcnt(0)\n"
    "s_nop 0\n"
    "v_mfma_f32_16x16x32_bf16 v[128:131], a[0:3], v[40:43], 0\n"
    "s_mov_b32 s49, s62\n"
    "s_add_u32 s62, s62, 2048\n"
    "v_mfma_f32_16x16x32_bf16 v[132:135], a[4:7], v[40:43], 0\n"
    "buffer_load_dwordx4 v[96:99], v14, s[40:43], s49 offen offset:0 sc1\n"
    "v_mfma_f32_16x16x32_bf16 v[136:139], a[8:11], v[40:43], 0\n"
    "v_mfma_f32_16x16x32_bf16 v[140:143], a[12:15], v[40:43], 0\n"
    "buffer_load_dwordx4 v[100:103], v14, s[40:43], s49 offen offset:1024 sc1\n"
    "v_mfma_f32_16x16x32_bf16 v[144:147], a[0:3], v[44:47], 0\n"
    "v_mfma_f32_16x16x32_bf16 v[148:151], a[4:7], v[44:47], 0\n"
    "s_waitcnt vmcnt(12)\n"
    "v_swap_b32 v50, v52\n"
    "v_swap_b32 v51, v53\n"
    "v_mfma_f32_16x16x32_bf16 v[152:155], a[8:11], v[44:47], 0\n"
    "v_mfma_f32_16x16x32_bf16 v[156:159], a[12:15], v[44:47], 0\n"
    "v_mfma_f32_16x16x32_bf16 v[128:131], a[16:19], v[48:51], v[128:131]\n"
    "s_mov_b32 s49, s62\n"
    "s_add_u32 s62, s62, 2048\n"
    "v_mfma_f32_16x16x32_bf16 v[132:135], a[20:23], v[48:51], v[132:135]\n"
    "buffer_load_dwordx4 v[40:43], v14, s[40:43], s49 offen offset:0 sc1\n"
    "v_mfma_f32_16x16x32_bf16 v[136:139], a[24:27], v[48:51], v[136:139]\n"
    "v_mfma_f32_16x16x32_bf16 v[140:143], a[28:31], v[48:51], v[140:143]\n"
    "buffer_load_dwordx4 v[44:47], v14, s[40:43], s49 offen offset:1024 sc1\n"
    "v_mfma_f32_16x16x32_bf16 v[144:147], a[16:19], v[52:55], v[144:147]\n"
    "v_mfma_f32_16x16x32_bf16 v[148:151], a[20:23], v[52:55], v[148:151]\n"
    "s_waitcnt vmcnt(12)\n"
    "v_swap_b32 v58, v60\n"
    "v_swap_b32 v59, v61\n"
    "v_mfma_f32_16x16x32_bf16 v[152:155], a[24:27], v[52:55], v[152:155]\n"
    "v_mfma_f32_16x16x32_bf16 v[156:159], a[28:31], v[52:55], v[156:159]\n"
    "v_mfma_f32_16x16x32_bf16 v[128:131], a[32:35], v[56:59], v[128:131]\n"
    "s_mov_b32 s49, s62\n"
    "s_add_u32 s62, s62, 2048\n"
    "v_mfma_f32_16x16x32_bf16 v[132:135], a[36:39], v[56:59], v[132:135]\n"
    "buffer_load_dwordx4 v[48:51], v14, s[40:43], s49 offen offset:0 sc1\n"
    "v_mfma_f32_16x16x32_bf16 v[136:139], a[40:43], v[56:59], v[136:139]\n"
    "v_mfma_f32_16x16x32_bf16 v[140:143], a[44:47], v[56:59], v[140:143]\n"
    "buffer_load_dwordx4 v[52:55], v14, s[40:43], s49 offen offset:1024 sc1\n"
    "v_mfma_f32_16x16x32_bf16 v[144:147], a[32:35], v[60:63], v[144:147]\n"
    "v_mfma_f32_16x16x32_bf16 v[148:151], a[36:39], v[60:63], v[148:151]\n"
    "s_waitcnt vmcnt(12)\n"
    "v_swap_b32 v66, v68\n"
    "v_swap_b32 v67, v69\n"
    "v_mfma_f32_16x16x32_bf16 v[152:155], a[40:43], v[60:63], v[152:155]\n"
    "v_mfma_f32_16x16x32_bf16 v[156:159], a[44:47], v[60:63], v[156:159]\n"
    "v_mfma_f32_16x16x32_bf16 v[128:131], a[48:51], v[64:67], v[128:131]\n"
    "s_mov_b32 s49, s62\n"
    "s_add_u32 s62, s62, 2048\n"
    "v_mfma_f32_16x16x32_bf16 v[132:135], a[52:55], v[64:67], v[132:135]\n"
    "buffer_load_dwordx4 v[56:59], v14, s[40:43], s49 offen offset:0 sc1\n"
    "v_mfma_f32_16x16x32_bf16 v[136:139], a[56:59], v[64:67], v[136:139]\n"
    "v_mfma_f32_16x16x32_bf16 v[140:143], a[60:63], v[64:67], v[140:143]\n"
    "buffer_load_dwordx4 v[60:63], v14, s[40:43], s49 offen offset:1024 sc1\n"
    "v_mfma_f32_16x16x32_bf16 v[144:147], a[48:51], v[68:71], v[144:147]\n"
    "v_mfma_f32_16x16x32_bf16 v[148:151], a[52:55], v[68:71], v[148:151]\n"
    "s_waitcnt vmcnt(12)\n"
    "v_swap_b32 v74, v76\n"
    "v_swap_b32 v75, v77\n"
    "v_mfma_f32_16x16x32_bf16 v[152:155], a[56:59], v[68:71], v[152:155]\n"
    "v_mfma_f32_16x16x32_bf16 v[156:159], a[60:63], v[68:71], v[156:159]\n"
    "v_mfma_f32_16x16x32_bf16 v[128:131], a[64:67], v[72:75], v[128:131]\n"
    "s_mov_b32 s49, s62\n"
    "s_add_u32 s62, s62, 2048\n"
    "v_mfma_f32_16x16x32_bf16 v[132:135], a[68:71], v[72:75], v[132:135]\n"
    "buffer_load_dwordx4 v[64:67], v14, s[40:43], s49 offen offset:0 sc1\n"
    "v_mfma_f32_16x16x32_bf16 v[136:139], a[72:75], v[72:75], v[136:139]\n"
    "v_mfma_f32_16x16x32_bf16 v[140:143], a[76:79], v[72:75], v[140:143]\n"
    "buffer_load_dwordx4 v[68:71], v14, s[40:43], s49 offen offset:1024 sc1\n"
    "v_mfma_f32_16x16x32_bf16 v[144:147], a[64:67], v[76:79], v[144:147]\n"
    "v_mfma_f32_16x16x32_bf16 v[148:151], a[68:71], v[76:79], v[148:151]\n"
    "s_waitcnt vmcnt(12)\n"
    "v_swap_b32 v82, v84\n"
    "v_swap_b32 v83, v85\n"
    "v_mfma_f32_16x16x32_bf16 v[152:155], a[72:75], v[76:79], v[152:155]\n"
    "v_mfma_f32_16x16x32_bf16 v[156:159], a[76:79], v[76:79], v[156:159]\n"
    "v_mfma_f32_16x16x32_bf16 v[128:131], a[80:83], v[80:83], v[128:131]\n"
    "s_mov_b32 s49, s62\n"
    "s_add_u32 s62, s62, 2048\n"
    "v_mfma_f32_16x16x32_bf16 v[132:135], a[84:87], v[80:83], v[132:135]\n"
    "buffer_load_dwordx4 v[72:75], v14, s[40:43], s49 offen offset:0 sc1\n"
    "v_mfma_f32_16x16x32_bf16 v[136:139], a[88:91], v[80:83], v[136:139]\n"
    "v_mfma_f32_16x16x32_bf16 v[140:143], a[92:95], v[80:83], v[140:143]\n"
    "buffer_load_dwordx4 v[76:79], v14, s[40:43], s49 offen offset:1024 sc1\n"
    "v_mfma_f32_16x16x32_bf16 v[144:147], a[80:83], v[84:87], v[144:147]\n"
    "v_mfma_f32_16x16x32_bf16 v[148:151], a[84:87], v[84:87], v[148:151]\n"
    "s_waitcnt vmcnt(12)\n"
    "v_swap_b32 v90, v92\n"
    "v_swap_b32 v91, v93\n"
    "v_mfma_f32_16x16x32_bf16 v[152:155], a[88:91], v[84:87], v[152:155]\n"
    "v_mfma_f32_16x16x32_bf16 v[156:159], a[92:95], v[84:87], v[156:159]\n"
    "v_mfma_f32_16x16x32_bf16 v[128:131], a[96:99], v[88:91], v[128:131]\n"
    "s_mov_b32 s49, s62\n"
    "s_add_u32 s62, s62, 2048\n"
    "v_mfma_f32_16x16x32_bf16 v[132:135], a[100:103], v[88:91], v[132:135]\n"
    "buffer_load_dwordx4 v[80:83], v14, s[40:43], s49 offen offset:0 sc1\n"
    "v_mfma_f32_16x16x32_bf16 v[136:139], a[104:107], v[88:91], v[136:139]\n"
    "v_mfma_f32_16x16x32_bf16 v[140:143], a[108:111], v[88:91], v[140:143]\n"
    "buffer_load_dwordx4 v[84:87], v14, s[40:43], s49 offen offset:1024 sc1\n"
    "v_mfma_f32_16x16x32_bf16 v[144:147], a[96:99], v[92:95], v[144:147]\n"
    "v_mfma_f32_16x16x32_bf16 v[148:151], a[100:103], v[92:95], v[148:151]\n"
    "s_waitcnt vmcnt(12)\n"
    "v_swap_b32 v98, v100\n"
    "v_swap_b32 v99, v101\n"
    "v_mfma_f32_16x16x32_bf16 v[152:155], a[104:107], v[92:95], v[152:155]\n"
    "v_mfma_f32_16x16x32_bf16 v[156:159], a[108:111], v[92:95], v[156:159]\n"
    "v_mfma_f32_16x16x32_bf16 v[128:131], a[112:115], v[96:99], v[128:131]\n"
    "s_mov_b32 s49, s62\n"
    "s_add_u32 s62, s62, 2048\n"
    "v_mfma_f32_16x16x32_bf16 v[132:135], a[116:119], v[96:99], v[132:135]\n"
    "buffer_load_dwordx4 v[88:91], v14, s[40:43], s49 offen offset:0 sc1\n"
    "v_mfma_f32_16x16x32_bf16 v[136:139], a[120:123], v[96:99], v[136:139]\n"
    "v_mfma_f32_16x16x32_bf16 v[140:143], a[124:127], v[96:99], v[140:143]\n"
    "buffer_load_dwordx4 v[92:95], v14, s[40:43], s49 offen offset:1024 sc1\n"
    "v_mfma_f32_16x16x32_bf16 v[144:147], a[112:115], v[100:103], v[144:147]\n"
    "v_mfma_f32_16x16x32_bf16 v[148:151], a[116:119], v[100:103], v[148:151]\n"
    "s_waitcnt vmcnt(12)\n"
    "v_swap_b32 v42, v44\n"
    "v_swap_b32 v43, v45\n"
    "v_mfma_f32_16x16x32_bf16 v[152:155], a[120:123], v[100:103], v[152:155]\n"
    "v_mfma_f32_16x16x32_bf16 v[156:159], a[124:127], v[100:103], v[156:159]\n"
    "v_mfma_f32_16x16x32_bf16 v[128:131], a[128:131], v[40:43], v[128:131]\n"
    "s_mov_b32 s49, s62\n"
    "s_add_u32 s62, s62, 2048\n"
    "v_mfma_f32_16x16x32_bf16 v[132:135], a[132:135], v[40:43], v[132:135]\n"
    "buffer_load_dwordx4 v[96:99], v14, s[40:43], s49 offen offset:0 sc1\n"
    "v_mfma_f32_16x16x32_bf16 v[136:139], a[136:139], v[40:43], v[136:139]\n"
    "v_mfma_f32_16x16x32_bf16 v[140:143], a[140:143], v[40:43], v[140:143]\n"
    "buffer_load_dwordx4 v[100:103], v14, s[40:43], s49 offen offset:1024 sc1\n"
    "v_mfma_f32_16x16x32_bf16 v[144:147], a[128:131], v[44:47], v[144:147]\n"
    "v_mfma_f32_16x16x32_bf16 v[148:151], a[132:135], v[44:47], v[148:151]\n"
    "s_waitcnt vmcnt(12)\n"
    "v_swap_b32 v50, v52\n"
    "v_swap_b32 v51, v53\n"
    "v_mfma_f32_16x16x32_bf16 v[152:155], a[136:139], v[44:47], v[152:155]\n"
    "v_mfma_f32_16x16x32_bf16 v[156:159], a[140:143], v[44:47], v[156:159]\n"
    "v_mfma_f32_16x16x32_bf16 v[128:131], a[144:147], v[48:51], v[128:131]\n"
    "s_mov_b32 s49, s62\n"
    "s_add_u32 s62, s62, 2048\n"
    "v_mfma_f32_16x16x32_bf16 v[132:135], a[148:151], v[48:51], v[132:135]\n"
    "buffer_load_dwordx4 v[40:43], v14, s[40:43], s49 offen offset:0 sc1\n"
    "v_mfma_f32_16x16x32_bf16 v[136:139], a[152:155], v[48:51], v[136:139]\n"
    "v_mfma_f32_16x16x32_bf16 v[140:143], a[156:159], v[48:51], v[140:143]\n"
    "buffer_load_dwordx4 v[44:47], v14, s[40:43], s49 offen offset:1024 sc1\n"
    "v_mfma_f32_16x16x32_bf16 v[144:147], a[144:147], v[52:55], v[144:147]\n"
    "v_mfma_f32_16x16x32_bf16 v[148:151], a[148:151], v[52:55], v[148:151]\n"
    "s_waitcnt vmcnt(12)\n"
    "v_swap_b32 v58, v60\n"
    "v_swap_b32 v59, v61\n"
    "v_mfma_f32_16x16x32_bf16 v[152:155], a[152:155], v[52:55], v[152:155]\n"
    "v_mfma_f32_16x16x32_bf16 v[156:159], a[156:159], v[52:55], v[156:159]\n"
    "v_mfma_f32_16x16x32_bf16 v[128:131], a[160:163], v[56:59], v[128:131]\n"
    "s_mov_b32 s49, s62\n"
    "s_add_u32 s62, s62, 2048\n"
    "v_mfma_f32_16x16x32_bf16 v[132:135], a[164:167], v[56:59], v[132:135]\n"
    "buffer_load_dwordx4 v[48:51], v14, s[40:43], s49 offen offset:0 sc1\n"
    "v_mfma_f32_16x16x32_bf16 v[136:139], a[168:171], v[56:59], v[136:139]\n"
    "v_mfma_f32_16x16x32_bf16 v[140:143], a[172:175], v[56:59], v[140:143]\n"
    "buffer_load_dwordx4 v[52:55], v14, s[40:43], s49 offen offset:1024 sc1\n"
    "v_mfma_f32_16x16x32_bf16 v[144:147], a[160:163], v[60:63], v[144:147]\n"
    "v_mfma_f32_16x16x32_bf16 v[148:151], a[164:167], v[60:63], v[148:151]\n"
    "s_waitcnt vmcnt(12)\n"
    "v_swap_b32 v66, v68\n"
    "v_swap_b32 v67, v69\n"
    "v_mfma_f32_16x16x32_bf16 v[152:155], a[168:171], v[60:63], v[152:155]\n"
    "v_mfma_f32_16x16x32_bf16 v[156:159], a[172:175], v[60:63], v[156:159]\n"
    "v_mfma_f32_16x16x32_bf16 v[128:131], a[176:179], v[64:67], v[128:131]\n"
    "s_mov_b32 s49, s62\n"
    "s_add_u32 s62, s62, 2048\n"
    "v_mfma_f32_16x16x32_bf16 v[132:135], a[180:183], v[64:67], v[132:135]\n"
    "buffer_load_dwordx4 v[56:59], v14, s[40:43], s49 offen offset:0 sc1\n"
    "v_mfma_f32_16x16x32_bf16 v[136:139], a[184:187], v[64:67], v[136:139]\n"
    "v_mfma_f32_16x16x32_bf16 v[140:143], a[188:191], v[64:67], v[140:143]\n"
    "buffer_load_dwordx4 v[60:63], v14, s[40:43], s49 offen offset:1024 sc1\n"
    "v_mfma_f32_16x16x32_bf16 v[144:147], a[176:179], v[68:71], v[144:147]\n"
    "v_mfma_f32_16x16x32_bf16 v[148:151], a[180:183], v[68:71], v[148:151]\n"
    "s_waitcnt vmcnt(12)\n"
    "v_swap_b32 v74, v76\n"
    "v_swap_b32 v75, v77\n"
    "v_mfma_f32_16x16x32_bf16 v[152:155], a[184:187], v[68:71], v[152:155]\n"
    "v_mfma_f32_16x16x32_bf16 v[156:159], a[188:191], v[68:71], v[156:159]\n"
    "v_mfma_f32_16x16x32_bf16 v[128:131], a[192:195], v[72:75], v[128:131]\n"
    "s_mov_b32 s49, s63\n"
    "s_add_u32 s63, s63, 2048\n"
    "v_mfma_f32_16x16x32_bf16 v[132:135], a[196:199], v[72:75], v[132:135]\n"
    "buffer_load_dwordx4 v[64:67], v14, s[44:47], s49 offen offset:0 sc1\n"
    "v_mfma_f32_16x16x32_bf16 v[136:139], a[200:203], v[72:75], v[136:139]\n"
    "v_mfma_f32_16x16x32_bf16 v[140:143], a[204:207], v[72:75], v[140:143]\n"
    "buffer_load_dwordx4 v[68:71], v14, s[44:47], s49 offen offset:1024 sc1\n"
    "v_mfma_f32_16x16x32_bf16 v[144:147], a[192:195], v[76:79], v[144:147]\n"
    "v_mfma_f32_16x16x32_bf16 v[148:151], a[196:199], v[76:79], v[148:151]\n"
    "s_waitcnt vmcnt(12)\n"
    "v_swap_b32 v82, v84\n"
    "v_swap_b32 v83, v85\n"
    "v_mfma_f32_16x16x32_bf16 v[152:155], a[200:203], v[76:79], v[152:155]\n"
    "v_mfma_f32_16x16x32_bf16 v[156:159], a[204:207], v[76:79], v[156:159]\n"
    "v_mfma_f32_16x16x32_bf16 v[128:131], a[208:211], v[80:83], v[128:131]\n"
    "s_mov_b32 s49, s63\n"
    "s_add_u32 s63, s63, 2048\n"
    "v_mfma_f32_16x16x32_bf16 v[132:135], a[212:215], v[80:83], v[132:135]\n"
    "buffer_load_dwordx4 v[72:75], v14, s[44:47], s49 offen offset:0 sc1\n"
    "v_mfma_f32_16x16x32_bf16 v[136:139], a[216:219], v[80:83], v[136:139]\n"
    "v_mfma_f32_16x16x32_bf16 v[140:143], a[220:223], v[80:83], v[140:143]\n"
    "buffer_load_dwordx4 v[76:79], v14, s[44:47], s49 offen offset:1024 sc1\n"
    "v_mfma_f32_16x16x32_bf16 v[144:147], a[208:211], v[84:87], v[144:147]\n"
    "v_mfma_f32_16x16x32_bf16 v[148:151], a[212:215], v[84:87], v[148:151]\n"
    "s_waitcnt vmcnt(12)\n"
    "v_swap_b32 v90, v92\n"
    "v_swap_b32 v91, v93\n"
    "v_mfma_f32_16x16x32_bf16 v[152:155], a[216:219], v[84:87], v[152:155]\n"
    "v_mfma_f32_16x16x32_bf16 v[156:159], a[220:223], v[84:87], v[156:159]\n"
    "v_mfma_f32_16x16x32_bf16 v[128:131], a[224:227], v[88:91], v[128:131]\n"
    "s_mov_b32 s49, s63\n"
    "s_add_u32 s63, s63, 2048\n"
    "v_mfma_f32_16x16x32_bf16 v[132:135], a[228:231], v[88:91], v[132:135]\n"
    "buffer_load_dwordx4 v[80:83], v14, s[44:47], s49 offen offset:0 sc1\n"
    "v_mfma_f32_16x16x32_bf16 v[136:139], a[232:235], v[88:91], v[136:139]\n"
    "v_mfma_f32_16x16x32_bf16 v[140:143], a[236:239], v[88:91], v[140:143]\n"
    "buffer_load_dwordx4 v[84:87], v14, s[44:47], s49 offen offset:1024 sc1\n"
    "v_mfma_f32_16x16x32_bf16 v[144:147], a[224:227], v[92:95], v[144:147]\n"
    "v_mfma_f32_16x16x32_bf16 v[148:151], a[228:231], v[92:95], v[148:151]\n"
    "s_waitcnt vmcnt(12)\n"
    "v_swap_b32 v98, v100\n"
    "v_swap_b32 v99, v101\n"
    "v_mfma_f32_16x16x32_bf16 v[152:155], a[232:235], v[92:95], v[152:155]\n"
    "v_mfma_f32_16x16x32_bf16 v[156:159], a[236:239], v[92:95], v[156:159]\n"
    "v_mfma_f32_16x16x32_bf16 v[128:131], a[240:243], v[96:99], v[128:131]\n"
    "s_mov_b32 s49, s63\n"
    "s_add_u32 s63, s63, 2048\n"
    "v_mfma_f32_16x16x32_bf16 v[132:135], a[244:247], v[96:99], v[132:135]\n"
    "buffer_load_dwordx4 v[88:91], v14, s[44:47], s49 offen offset:0 sc1\n"
    "v_mfma_f32_16x16x32_bf16 v[136:139], a[248:251], v[96:99], v[136:139]\n"
    "v_mfma_f32_16x16x32_bf16 v[140:143], a[252:255], v[96:99], v[140:143]\n"
    "buffer_load_dwordx4 v[92:95], v14, s[44:47], s49 offen offset:1024 sc1\n"
    "v_mfma_f32_16x16x32_bf16 v[144:147], a[240:243], v[100:103], v[144:147]\n"
    "v_mfma_f32_16x16x32_bf16 v[148:151], a[244:247], v[100:103], v[148:151]\n"
    "s_waitcnt vmcnt(12)\n"
    "v_swap_b32 v42, v44\n"
    "v_swap_b32 v43, v45\n"
    "v_mfma_f32_16x16x32_bf16 v[152:155], a[248:251], v[100:103], v[152:155]\n"
    "v_mfma_f32_16x16x32_bf16 v[156:159], a[252:255], v[100:103], v[156:159]\n"
    "v_mfma_f32_16x16x32_bf16 v[128:131], v[192:195], v[40:43], v[128:131]\n"
    "s_mov_b32 s49, s63\n"
    "s_add_u32 s63, s63, 2048\n"
    "v_mfma_f32_16x16x32_bf16 v[132:135], v[196:199], v[40:43], v[132:135]\n"
    "buffer_load_dwordx4 v[96:99], v14, s[44:47], s49 offen offset:0 sc1\n"
    "v_mfma_f32_16x16x32_bf16 v[136:139], v[200:203], v[40:43], v[136:139]\n"
    "v_mfma_f32_16x16x32_bf16 v[140:143], v[204:207], v[40:43], v[140:143]\n"
    "buffer_load_dwordx4 v[100:103], v14, s[44:47], s49 offen offset:1024 sc1\n"
    "v_mfma_f32_16x16x32_bf16 v[144:147], v[192:195], v[44:47], v[144:147]\n"
    "v_mfma_f32_16x16x32_bf16 v[148:151], v[196:199], v[44:47], v[148:151]\n"
    "s_waitcnt vmcnt(12)\n"
    "v_swap_b32 v50, v52\n"
    "v_swap_b32 v51, v53\n"
    "v_mfma_f32_16x16x32_bf16 v[152:155], v[200:203], v[44:47], v[152:155]\n"
    "v_mfma_f32_16x16x32_bf16 v[156:159], v[204:207], v[44:47], v[156:159]\n"
    "v_mfma_f32_16x16x32_bf16 v[128:131], v[208:211], v[48:51], v[128:131]\n"
    "s_mov_b32 s49, s63\n"
    "s_add_u32 s63, s63, 2048\n"
    "v_mfma_f32_16x16x32_bf16 v[132:135], v[212:215], v[48:51], v[132:135]\n"
    "buffer_load_dwordx4 v[40:43], v14, s[44:47], s49 offen offset:0 sc1\n"
    "v_mfma_f32_16x16x32_bf16 v[136:139], v[216:219], v[48:51], v[136:139]\n"
    "v_mfma_f32_16x16x32_bf16 v[140:143], v[220:223], v[48:51], v[140:143]\n"
    "buffer_load_dwordx4 v[44:47], v14, s[44:47], s49 offen offset:1024 sc1\n"
    "v_mfma_f32_16x16x32_bf16 v[144:147], v[208:211], v[52:55], v[144:147]\n"
    "v_mfma_f32_16x16x32_bf16 v[148:151], v[212:215], v[52:55], v[148:151]\n"
    "s_waitcnt vmcnt(12)\n"
    "v_swap_b32 v58, v60\n"
    "v_swap_b32 v59, v61\n"
    "v_mfma_f32_16x16x32_bf16 v[152:155], v[216:219], v[52:55], v[152:155]\n"
    "v_mfma_f32_16x16x32_bf16 v[156:159], v[220:223], v[52:55], v[156:159]\n"
    "v_mfma_f32_16x16x32_bf16 v[128:131], v[224:227], v[56:59], v[128:131]\n"
    "s_mov_b32 s49, s63\n"
    "s_add_u32 s63, s63, 2048\n"
    "v_mfma_f32_16x16x32_bf16 v[132:135], v[228:231], v[56:59], v[132:135]\n"
    "buffer_load_dwordx4 v[48:51], v14, s[44:47], s49 offen offset:0 sc1\n"
    "v_mfma_f32_16x16x32_bf16 v[136:139], v[232:235], v[56:59], v[136:139]\n"
    "v_mfma_f32_16x16x32_bf16 v[140:143], v[236:239], v[56:59], v[140:143]\n"
    "buffer_load_dwordx4 v[52:55], v14, s[44:47], s49 offen offset:1024 sc1\n"
    "v_mfma_f32_16x16x32_bf16 v[144:147], v[224:227], v[60:63], v[144:147]\n"
    "v_mfma_f32_16x16x32_bf16 v[148:151], v[228:231], v[60:63], v[148:151]\n"
    "s_waitcnt vmcnt(12)\n"
    "v_swap_b32 v66, v68\n"
    "v_swap_b32 v67, v69\n"
    "v_mfma_f32_16x16x32_bf16 v[152:155], v[232:235], v[60:63], v[152:155]\n"
    "v_mfma_f32_16x16x32_bf16 v[156:159], v[236:239], v[60:63], v[156:159]\n"
    "v_mfma_f32_16x16x32_bf16 v[160:163], a[0:3], v[64:67], 0\n"
    "s_mov_b32 s49, s63\n"
    "s_add_u32 s63, s63, 2048\n"
    "v_mfma_f32_16x16x32_bf16 v[164:167], a[4:7], v[64:67], 0\n"
    "buffer_load_dwordx4 v[56:59], v14, s[44:47], s49 offen offset:0 sc1\n"
    "v_mfma_f32_16x16x32_bf16 v[168:171], a[8:11], v[64:67], 0\n"
    "v_mfma_f32_16x16x32_bf16 v[172:175], a[12:15], v[64:67], 0\n"
    "buffer_load_dwordx4 v[60:63], v14, s[44:47], s49 offen offset:1024 sc1\n"
    "v_mfma_f32_16x16x32_bf16 v[176:179], a[0:3], v[68:71], 0\n"
    "v_mfma_f32_16x16x32_bf16 v[180:183], a[4:7], v[68:71], 0\n"
    "s_waitcnt vmcnt(12)\n"
    "v_swap_b32 v74, v76\n"
    "v_swap_b32 v75, v77\n"
    "v_mfma_f32_16x16x32_bf16 v[184:187], a[8:11], v[68:71], 0\n"
    "v_mfma_f32_16x16x32_bf16 v[188:191], a[12:15], v[68:71], 0\n"
    "v_mfma_f32_16x16x32_bf16 v[160:163], a[16:19], v[72:75], v[160:163]\n"
    "s_mov_b32 s49, s63\n"
    "s_add_u32 s63, s63, 2048\n"
    "v_mfma_f32_16x16x32_bf16 v[164:167], a[20:23], v[72:75], v[164:167]\n"
    "buffer_load_dwordx4 v[64:67], v14, s[44:47], s49 offen offset:0 sc1\n"
    "v_mfma_f32_16x16x32_bf16 v[168:171], a[24:27], v[72:75], v[168:171]\n"
    "ds_write_b128 v17, v[128:131] offset:0\n"
    "v_mfma_f32_16x16x32_bf16 v[172:175], a[28:31], v[72:75], v[172:175]\n"
    "buffer_load_dwordx4 v[68:71], v14, s[44:47], s49 offen offset:1024 sc1\n"
    "v_mfma_f32_16x16x32_bf16 v[176:179], a[16:19], v[76:79], v[176:179]\n"
    "v_mfma_f32_16x16x32_bf16 v[180:183], a[20:23], v[76:79], v[180:183]\n"
    "s_waitcnt vmcnt(12)\n"
    "v_swap_b32 v82, v84\n"
    "v_swap_b32 v83, v85\n"
    "v_mfma_f32_16x16x32_bf16 v[184:187], a[24:27], v[76:79], v[184:187]\n"
    "v_mfma_f32_16x16x32_bf16 v[188:191], a[28:31], v[76:79], v[188:191]\n"
    "v_mfma_f32_16x16x32_bf16 v[160:163], a[32:35], v[80:83], v[160:163]\n"
    "s_mov_b32 s49, s63\n"
    "s_add_u32 s63, s63, 2048\n"
    "v_mfma_f32_16x16x32_bf16 v[164:167], a[36:39], v[80:83], v[164:167]\n"
    "buffer_load_dwordx4 v[72:75], v14, s[44:47], s49 offen offset:0 sc1\n"
    "v_mfma_f32_16x16x32_bf16 v[168:171], a[40:43], v[80:83], v[168:171]\n"
    "ds_write_b128 v18, v[132:135] offset:0\n"
    "v_mfma_f32_16x16x32_bf16 v[172:175], a[44:47], v[80:83], v[172:175]\n"
    "buffer_load_dwordx4 v[76:79], v14, s[44:47], s49 offen offset:1024 sc1\n"
    "v_mfma_f32_16x16x32_bf16 v[176:179], a[32:35], v[84:87], v[176:179]\n"
    "v_mfma_f32_16x16x32_bf16 v[180:183], a[36:39], v[84:87], v[180:183]\n"
    "s_waitcnt vmcnt(12)\n"
    "v_swap_b32 v90, v92\n"
    "v_swap_b32 v91, v93\n"
    "v_mfma_f32_16x16x32_bf16 v[184:187], a[40:43], v[84:87], v[184:187]\n"
    "v_mfma_f32_16x16x32_bf16 v[188:191], a[44:47], v[84:87], v[188:191]\n"
    "v_mfma_f32_16x16x32_bf16 v[160:163], a[48:51], v[88:91], v[160:163]\n"
    "s_mov_b32 s49, s63\n"
    "s_add_u32 s63, s63, 2048\n"
    "v_mfma_f32_16x16x32_bf16 v[164:167], a[52:55], v[88:91], v[164:167]\n"
    "buffer_load_dwordx4 v[80:83], v14, s[44:47], s49 offen offset:0 sc1\n"
    "v_mfma_f32_16x16x32_bf16 v[168:171], a[56:59], v[88:91], v[168:171]\n"
    "ds_write_b128 v17, v[136:139] offset:128\n"
    "v_mfma_f32_16x16x32_bf16 v[172:175], a[60:63], v[88:91], v[172:175]\n"
    "buffer_load_dwordx4 v[84:87], v14, s[44:47], s49 offen offset:1024 sc1\n"
    "v_mfma_f32_16x16x32_bf16 v[176:179], a[48:51], v[92:95], v[176:179]\n"
    "v_mfma_f32_16x16x32_bf16 v[180:183], a[52:55], v[92:95], v[180:183]\n"
    "s_waitcnt vmcnt(12)\n"
    "v_swap_b32 v98, v100\n"
    "v_swap_b32 v99, v101\n"
    "v_mfma_f32_16x16x32_bf16 v[184:187], a[56:59], v[92:95], v[184:187]\n"
    "v_mfma_f32_16x16x32_bf16 v[188:191], a[60:63], v[92:95], v[188:191]\n"
    "v_mfma_f32_16x16x32_bf16 v[160:163], a[64:67], v[96:99], v[160:163]\n"
    "s_mov_b32 s49, s63\n"
    "s_add_u32 s63, s63, 2048\n"
    "v_mfma_f32_16x16x32_bf16 v[164:167], a[68:71], v[96:99], v[164:167]\n"
    "buffer_load_dwordx4 v[88:91], v14, s[44:47], s49 offen offset:0 sc1\n"
    "v_mfma_f32_16x16x32_bf16 v[168:171], a[72:75], v[96:99], v[168:171]\n"
    "ds_write_b128 v18, v[140:143] offset:128\n"
    "v_mfma_f32_16x16x32_bf16 v[172:175], a[76:79], v[96:99], v[172:175]\n"
    "buffer_load_dwordx4 v[92:95], v14, s[44:47], s49 offen offset:1024 sc1\n"
    "v_mfma_f32_16x16x32_bf16 v[176:179], a[64:67], v[100:103], v[176:179]\n"
    "v_mfma_f32_16x16x32_bf16 v[180:183], a[68:71], v[100:103], v[180:183]\n"
    "s_waitcnt vmcnt(12)\n"
    "v_swap_b32 v42, v44\n"
    "v_swap_b32 v43, v45\n"
    "v_mfma_f32_16x16x32_bf16 v[184:187], a[72:75], v[100:103], v[184:187]\n"
    "v_mfma_f32_16x16x32_bf16 v[188:191], a[76:79], v[100:103], v[188:191]\n"
    "v_mfma_f32_16x16x32_bf16 v[160:163], a[80:83], v[40:43], v[160:163]\n"
    "s_mov_b32 s49, s63\n"
    "s_add_u32 s63, s63, 2048\n"
    "v_mfma_f32_16x16x32_bf16 v[164:167], a[84:87], v[40:43], v[164:167]\n"
    "buffer_load_dwordx4 v[96:99], v14, s[44:47], s49 offen offset:0 sc1\n"
    "v_mfma_f32_16x16x32_bf16 v[168:171], a[88:91], v[40:43], v[168:171]\n"
    "ds_write_b128 v17, v[144:147] offset:16384\n"
    "v_mfma_f32_16x16x32_bf16 v[172:175], a[92:95], v[40:43], v[172:175]\n"
    "buffer_load_dwordx4 v[100:103], v14, s[44:47], s49 offen offset:1024 sc1\n"
    "v_mfma_f32_16x16x32_bf16 v[176:179], a[80:83], v[44:47], v[176:179]\n"
    "v_mfma_f32_16x16x32_bf16 v[180:183], a[84:87], v[44:47], v[180:183]\n"
    "s_waitcnt vmcnt(12)\n"
    "v_swap_b32 v50, v52\n"
    "v_swap_b32 v51, v53\n"
    "v_mfma_f32_16x16x32_bf16 v[184:187], a[88:91], v[44:47], v[184:187]\n"
    "v_mfma_f32_16x16x32_bf16 v[188:191], a[92:95], v[44:47], v[188:191]\n"
    "v_mfma_f32_16x16x32_bf16 v[160:163], a[96:99], v[48:51], v[160:163]\n"
    "s_mov_b32 s49, s63\n"
    "s_add_u32 s63, s63, 2048\n"
    "v_mfma_f32_16x16x32_bf16 v[164:167], a[100:103], v[48:51], v[164:167]\n"
    "buffer_load_dwordx4 v[40:43], v14, s[44:47], s49 offen offset:0 sc1\n"
    "v_mfma_f32_16x16x32_bf16 v[168:171], a[104:107], v[48:51], v[168:171]\n"
    "ds_write_b128 v18, v[148:151] offset:16384\n"
    "v_mfma_f32_16x16x32_bf16 v[172:175], a[108:111], v[48:51], v[172:175]\n"
    "buffer_load_dwordx4 v[44:47], v14, s[44:47], s49 offen offset:1024 sc1\n"
    "v_mfma_f32_16x16x32_bf16 v[176:179], a[96:99], v[52:55], v[176:179]\n"
    "v_mfma_f32_16x16x32_bf16 v[180:183], a[100:103], v[52:55], v[180:183]\n"
    "s_waitcnt vmcnt(12)\n"
    "v_swap_b32 v58, v60\n"
    "v_swap_b32 v59, v61\n"
    "v_mfma_f32_16x16x32_bf16 v[184:187], a[104:107], v[52:55], v[184:187]\n"
    "v_mfma_f32_16x16x32_bf16 v[188:191], a[108:111], v[52:55], v[188:191]\n"
    "v_mfma_f32_16x16x32_bf16 v[160:163], a[112:115], v[56:59], v[160:163]\n"
    "s_mov_b32 s49, s63\n"
    "s_add_u32 s63, s63, 2048\n"
    "v_mfma_f32_16x16x32_bf16 v[164:167], a[116:119], v[56:59], v[164:167]\n"
    "buffer_load_dwordx4 v[48:51], v14, s[44:47], s49 offen offset:0 sc1\n"
    "v_mfma_f32_16x16x32_bf16 v[168:171], a[120:123], v[56:59], v[168:171]\n"
    "ds_write_b128 v17, v[152:155] offset:16512\n"
    "v_mfma_f32_16x16x32_bf16 v[172:175], a[124:127], v[56:59], v[172:175]\n"
    "buffer_load_dwordx4 v[52:55], v14, s[44:47], s49 offen offset:1024 sc1\n"
    "v_mfma_f32_16x16x32_bf16 v[176:179], a[112:115], v[60:63], v[176:179]\n"
    "v_mfma_f32_16x16x32_bf16 v[180:183], a[116:119], v[60:63], v[180:183]\n"
    "s_waitcnt vmcnt(12)\n"
    "v_swap_b32 v66, v68\n"
    "v_swap_b32 v67, v69\n"
    "v_mfma_f32_16x16x32_bf16 v[184:187], a[120:123], v[60:63], v[184:187]\n"
    "v_mfma_f32_16x16x32_bf16 v[188:191], a[124:127], v[60:63], v[188:191]\n"
    "v_mfma_f32_16x16x32_bf16 v[160:163], a[128:131], v[64:67], v[160:163]\n"
    "s_mov_b32 s49, s63\n"
    "s_add_u32 s63, s63, 2048\n"
    "v_mfma_f32_16x16x32_bf16 v[164:167], a[132:135], v[64:67], v[164:167]\n"
    "buffer_load_dwordx4 v[56:59], v14, s[44:47], s49 offen offset:0 sc1\n"
    "v_mfma_f32_16x16x32_bf16 v[168:171], a[136:139], v[64:67], v[168:171]\n"
    "ds_write_b128 v18, v[156:159] offset:16512\n"
    "v_mfma_f32_16x16x32_bf16 v[172:175], a[140:143], v[64:67], v[172:175]\n"
    "buffer_load_dwordx4 v[60:63], v14, s[44:47], s49 offen offset:1024 sc1\n"
    "v_mfma_f32_16x16x32_bf16 v[176:179], a[128:131], v[68:71], v[176:179]\n"
    "v_mfma_f32_16x16x32_bf16 v[180:183], a[132:135], v[68:71], v[180:183]\n"
    "s_waitcnt vmcnt(12)\n"
    "v_swap_b32 v74, v76\n"
    "v_swap_b32 v75, v77\n"
    "v_mfma_f32_16x16x32_bf16 v[184:187], a[136:139], v[68:71], v[184:187]\n"
    "v_mfma_f32_16x16x32_bf16 v[188:191], a[140:143], v[68:71], v[188:191]\n"
    "v_mfma_f32_16x16x32_bf16 v[160:163], a[144:147], v[72:75], v[160:163]\n"
    "s_mov_b32 s49, s63\n"
    "s_add_u32 s63, s63, 2048\n"
    "v_mfma_f32_16x16x32_bf16 v[164:167], a[148:151], v[72:75], v[164:167]\n"
    "buffer_load_dwordx4 v[64:67], v14, s[44:47], s49 offen offset:0 sc1\n"
    "v_mfma_f32_16x16x32_bf16 v[168:171], a[152:155], v[72:75], v[168:171]\n"
    "v_mfma_f32_16x16x32_bf16 v[172:175], a[156:159], v[72:75], v[172:175]\n"
    "buffer_load_dwordx4 v[68:71], v14, s[44:47], s49 offen offset:1024 sc1\n"
    "v_mfma_f32_16x16x32_bf16 v[176:179], a[144:147], v[76:79], v[176:179]\n"
    "v_mfma_f32_16x16x32_bf16 v[180:183], a[148:151], v[76:79], v[180:183]\n"
    "s_waitcnt vmcnt(12)\n"
    "v_swap_b32 v82, v84\n"
    "v_swap_b32 v83, v85\n"
    "v_mfma_f32_16x16x32_bf16 v[184:187], a[152:155], v[76:79], v[184:187]\n"
    "v_mfma_f32_16x16x32_bf16 v[188:191], a[156:159], v[76:79], v[188:191]\n"
    "v_mfma_f32_16x16x32_bf16 v[160:163], a[160:163], v[80:83], v[160:163]\n"
    "s_mov_b32 s49, s63\n"
    "s_add_u32 s63, s63, 2048\n"
    "v_mfma_f32_16x16x32_bf16 v[164:167], a[164:167], v[80:83], v[164:167]\n"
    "buffer_load_dwordx4 v[72:75], v14, s[44:47], s49 offen offset:0 sc1\n"
    "v_mfma_f32_16x16x32_bf16 v[168:171], a[168:171], v[80:83], v[168:171]\n"
    "v_mfma_f32_16x16x32_bf16 v[172:175], a[172:175], v[80:83], v[172:175]\n"
    "buffer_load_dwordx4 v[76:79], v14, s[44:47], s49 offen offset:1024 sc1\n"
    "v_mfma_f32_16x16x32_bf16 v[176:179], a[160:163], v[84:87], v[176:179]\n"
    "v_mfma_f32_16x16x32_bf16 v[180:183], a[164:167], v[84:87], v[180:183]\n"
    "s_waitcnt vmcnt(12)\n"
    "v_swap_b32 v90, v92\n"
    "v_swap_b32 v91, v93\n"
    "v_mfma_f32_16x16x32_bf16 v[184:187], a[168:171], v[84:87], v[184:187]\n"
    "v_mfma_f32_16x16x32_bf16 v[188:191], a[172:175], v[84:87], v[188:191]\n"
    "v_mfma_f32_16x16x32_bf16 v[160:163], a[176:179], v[88:91], v[160:163]\n"
    "s_mov_b32 s49, s63\n"
    "s_add_u32 s63, s63, 2048\n"
    "v_mfma_f32_16x16x32_bf16 v[164:167], a[180:183], v[88:91], v[164:167]\n"
    "buffer_load_dwordx4 v[80:83], v14, s[44:47], s49 offen offset:0 sc1\n"
    "v_mfma_f32_16x16x32_bf16 v[168:171], a[184:187], v[88:91], v[168:171]\n"
    "v_mfma_f32_16x16x32_bf16 v[172:175], a[188:191], v[88:91], v[172:175]\n"
    "buffer_load_dwordx4 v[84:87], v14, s[44:47], s49 offen offset:1024 sc1\n"
    "v_mfma_f32_16x16x32_bf16 v[176:179], a[176:179], v[92:95], v[176:179]\n"
    "v_mfma_f32_16x16x32_bf16 v[180:183], a[180:183], v[92:95], v[180:183]\n"
    "s_waitcnt vmcnt(12)\n"
    "v_swap_b32 v98, v100\n"
    "v_swap_b32 v99, v101\n"
    "v_mfma_f32_16x16x32_bf16 v[184:187], a[184:187], v[92:95], v[184:187]\n"
    "v_mfma_f32_16x16x32_bf16 v[188:191], a[188:191], v[92:95], v[188:191]\n"
    "v_mfma_f32_16x16x32_bf16 v[160:163], a[192:195], v[96:99], v[160:163]\n"
    "v_mfma_f32_16x16x32_bf16 v[164:167], a[196:199], v[96:99], v[164:167]\n"
    "buffer_load_dwordx4 v[104:107], v23, s[4:7], 0 offen\n"
    "v_mfma_f32_16x16x32_bf16 v[168:171], a[200:203], v[96:99], v[168:171]\n"
    "v_mfma_f32_16x16x32_bf16 v[172:175], a[204:207], v[96:99], v[172:175]\n"
    "buffer_load_dwordx4 v[108:111], v24, s[4:7], 0 offen\n"
    "v_mfma_f32_16x16x32_bf16 v[176:179], a[192:195], v[100:103], v[176:179]\n"
    "buffer_load_dwordx4 v[112:115], v25, s[20:23], 0 offen\n"
    "v_mfma_f32_16x16x32_bf16 v[180:183], a[196:199], v[100:103], v[180:183]\n"
    "s_waitcnt vmcnt(13)\n"
    "v_swap_b32 v42, v44\n"
    "v_swap_b32 v43, v45\n"
    "v_mfma_f32_16x16x32_bf16 v[184:187], a[200:203], v[100:103], v[184:187]\n"
    "v_mfma_f32_16x16x32_bf16 v[188:191], a[204:207], v[100:103], v[188:191]\n"
    "v_mfma_f32_16x16x32_bf16 v[160:163], a[208:211], v[40:43], v[160:163]\n"
    "v_mfma_f32_16x16x32_bf16 v[164:167], a[212:215], v[40:43], v[164:167]\n"
    "v_mfma_f32_16x16x32_bf16 v[168:171], a[216:219], v[40:43], v[168:171]\n"
    "v_mfma_f32_16x16x32_bf16 v[172:175], a[220:223], v[40:43], v[172:175]\n"
    "v_mfma_f32_16x16x32_bf16 v[176:179], a[208:211], v[44:47], v[176:179]\n"
    "v_mfma_f32_16x16x32_bf16 v[180:183], a[212:215], v[44:47], v[180:183]\n"
    "s_waitcnt vmcnt(11)\n"
    "v_swap_b32 v50, v52\n"
    "v_swap_b32 v51, v53\n"
    "v_mfma_f32_16x16x32_bf16 v[184:187], a[216:219], v[44:47], v[184:187]\n"
    "v_mfma_f32_16x16x32_bf16 v[188:191], a[220:223], v[44:47], v[188:191]\n"
    "v_mfma_f32_16x16x32_bf16 v[160:163], a[224:227], v[48:51], v[160:163]\n"
    "v_mfma_f32_16x16x32_bf16 v[164:167], a[228:231], v[48:51], v[164:167]\n"
    "v_mfma_f32_16x16x32_bf16 v[168:171], a[232:235], v[48:51], v[168:171]\n"
    "v_mfma_f32_16x16x32_bf16 v[172:175], a[236:239], v[48:51], v[172:175]\n"
    "v_mfma_f32_16x16x32_bf16 v[176:179], a[224:227], v[52:55], v[176:179]\n"
    "v_mfma_f32_16x16x32_bf16 v[180:183], a[228:231], v[52:55], v[180:183]\n"
    "s_waitcnt vmcnt(9)\n"
    "v_swap_b32 v58, v60\n"
    "v_swap_b32 v59, v61\n"
    "v_mfma_f32_16x16x32_bf16 v[184:187], a[232:235], v[52:55], v[184:187]\n"
    "v_mfma_f32_16x16x32_bf16 v[188:191], a[236:239], v[52:55], v[188:191]\n"
    "v_mfma_f32_16x16x32_bf16 v[160:163], a[240:243], v[56:59], v[160:163]\n"
    "v_mfma_f32_16x16x32_bf16 v[164:167], a[244:247], v[56:59], v[164:167]\n"
    "v_mfma_f32_16x16x32_bf16 v[168:171], a[248:251], v[56:59], v[168:171]\n"
    "v_mfma_f32_16x16x32_bf16 v[172:175], a[252:255], v[56:59], v[172:175]\n"
    "v_mfma_f32_16x16x32_bf16 v[176:179], a[240:243], v[60:63], v[176:179]\n"
    "v_mfma_f32_16x16x32_bf16 v[180:183], a[244:247], v[60:63], v[180:183]\n"
    "s_waitcnt vmcnt(7)\n"
    "v_swap_b32 v66, v68\n"
    "v_swap_b32 v67, v69\n"
    "v_mfma_f32_16x16x32_bf16 v[184:187], a[248:251], v[60:63], v[184:187]\n"
    "v_mfma_f32_16x16x32_bf16 v[188:191], a[252:255], v[60:63], v[188:191]\n"
    "v_mfma_f32_16x16x32_bf16 v[160:163], v[192:195], v[64:67], v[160:163]\n"
    "v_mfma_f32_16x16x32_bf16 v[164:167], v[196:199], v[64:67], v[164:167]\n"
    "v_mfma_f32_16x16x32_bf16 v[168:171], v[200:203], v[64:67], v[168:171]\n"
    "v_mfma_f32_16x16x32_bf16 v[172:175], v[204:207], v[64:67], v[172:175]\n"
    "v_mfma_f32_16x16x32_bf16 v[176:179], v[192:195], v[68:71], v[176:179]\n"
    "v_mfma_f32_16x16x32_bf16 v[180:183], v[196:199], v[68:71], v[180:183]\n"
    "s_waitcnt vmcnt(5)\n"
    "v_swap_b32 v74, v76\n"
    "v_swap_b32 v75, v77\n"
    "v_mfma_f32_16x16x32_bf16 v[184:187], v[200:203], v[68:71], v[184:187]\n"
    "v_mfma_f32_16x16x32_bf16 v[188:191], v[204:207], v[68:71], v[188:191]\n"
    "v_mfma_f32_16x16x32_bf16 v[160:163], v[208:211], v[72:75], v[160:163]\n"
    "v_mfma_f32_16x16x32_bf16 v[164:167], v[212:215], v[72:75], v[164:167]\n"
    "v_mfma_f32_16x16x32_bf16 v[168:171], v[216:219], v[72:75], v[168:171]\n"
    "v_mfma_f32_16x16x32_bf16 v[172:175], v[220:223], v[72:75], v[172:175]\n"
    "v_mfma_f32_16x16x32_bf16 v[176:179], v[208:211], v[76:79], v[176:179]\n"
    "v_mfma_f32_16x16x32_bf16 v[180:183], v[212:215], v[76:79], v[180:183]\n"
    "s_waitcnt vmcnt(3)\n"
    "v_swap_b32 v82, v84\n"
    "v_swap_b32 v83, v85\n"
    "v_mfma_f32_16x16x32_bf16 v[184:187], v[216:219], v[76:79], v[184:187]\n"
    "v_mfma_f32_16x16x32_bf16 v[188:191], v[220:223], v[76:79], v[188:191]\n"
    "v_mfma_f32_16x16x32_bf16 v[160:163], v[224:227], v[80:83], v[160:163]\n"
    "v_mfma_f32_16x16x32_bf16 v[164:167], v[228:231], v[80:83], v[164:167]\n"
    "v_mfma_f32_16x16x32_bf16 v[168:171], v[232:235], v[80:83], v[168:171]\n"
    "v_mfma_f32_16x16x32_bf16 v[172:175], v[236:239], v[80:83], v[172:175]\n"
    "v_mfma_f32_16x16x32_bf16 v[176:179], v[224:227], v[84:87], v[176:179]\n"
    "v_mfma_f32_16x16x32_bf16 v[180:183], v[228:231], v[84:87], v[180:183]\n"
    "v_mfma_f32_16x16x32_bf16 v[184:187], v[232:235], v[84:87], v[184:187]\n"
    "v_mfma_f32_16x16x32_bf16 v[188:191], v[236:239], v[84:87], v[188:191]\n"
    "s_waitcnt vmcnt(0)\n"
    "v_lshlrev_b32 v240, 16, v104\n"
    "v_and_b32 v241, 0xffff0000, v104\n"
    "v_lshlrev_b32 v242, 16, v105\n"
    "v_and_b32 v243, 0xffff0000, v105\n"
    "ds_write_b128 v19, v[160:163] offset:0\n"
    "v_lshlrev_b32 v244, 16, v106\n"
    "v_and_b32 v245, 0xffff0000, v106\n"
    "ds_write_b128 v20, v[164:167] offset:0\n"
    "v_lshlrev_b32 v246, 16, v107\n"
    "v_and_b32 v247, 0xffff0000, v107\n"
    "v_lshlrev_b32 v248, 16, v108\n"
    "ds_write_b128 v19, v[168:171] offset:128\n"
    "v_and_b32 v249, 0xffff0000, v108\n"
    "v_lshlrev_b32 v250, 16, v109\n"
    "ds_write_b128 v20, v[172:175] offset:128\n"
    "v_and_b32 v251, 0xffff0000, v109\n"
    "v_lshlrev_b32 v252, 16, v110\n"
    "v_and_b32 v253, 0xffff0000, v110\n"
    "ds_write_b128 v19, v[176:179] offset:16384\n"
    "v_lshlrev_b32 v254, 16, v111\n"
    "v_and_b32 v255, 0xffff0000, v111\n"
    "v_lshlrev_b32 v116, 16, v112\n"
    "ds_write_b128 v20, v[180:183] offset:16384\n"
    "v_and_b32 v117, 0xffff0000, v112\n"
    "v_lshlrev_b32 v118, 16, v113\n"
    "ds_write_b128 v19, v[184:187] offset:16512\n"
    "v_and_b32 v119, 0xffff0000, v113\n"
    "v_lshlrev_b32 v120, 16, v114\n"
    "v_and_b32 v121, 0xffff0000, v114\n"
    "ds_write_b128 v20, v[188:191] offset:16512\n"
    "v_lshlrev_b32 v122, 16, v115\n"
    "v_and_b32 v123, 0xffff0000, v115\n"
    "s_waitcnt lgkmcnt(0)\n"
    "s_barrier\n"
    "ds_read_b128 v[56:59], v21 offset:4096\n"
    "ds_read_b128 v[60:63], v22 offset:4096\n"
    "ds_read_b128 v[72:75], v21 offset:8192\n"
    "ds_read_b128 v[76:79], v22 offset:8192\n"
    "ds_read_b128 v[88:91], v21 offset:12288\n"
    "ds_read_b128 v[92:95], v22 offset:12288\n"
    "ds_read_b128 v[40:43], v21 offset:0\n"
    "ds_read_b128 v[44:47], v22 offset:0\n"
    "ds_read_b128 v[64:67], v21 offset:6144\n"
    "ds_read_b128 v[68:71], v22 offset:6144\n"
    "ds_read_b128 v[80:83], v21 offset:10240\n"
    "ds_read_b128 v[84:87], v22 offset:10240\n"
    "ds_read_b128 v[96:99], v21 offset:14336\n"
    "ds_read_b128 v[100:103], v22 offset:14336\n"
    "ds_read_b128 v[48:51], v21 offset:2048\n"
    "ds_read_b128 v[52:55], v22 offset:2048\n"
    "v_mov_b32 v124, 0xbfb8aa3b\n"
    "v_mov_b32 v125, 1.0\n"
    "s_waitcnt lgkmcnt(8)\n"
    "v_pk_add_f32 v[56:57], v[56:57], v[72:73]\n"
    "v_pk_add_f32 v[88:89], v[88:89], v[116:117]\n"
    "v_pk_add_f32 v[58:59], v[58:59], v[74:75]\n"
    "v_pk_add_f32 v[90:91], v[90:91], v[118:119]\n"
    "v_pk_add_f32 v[60:61], v[60:61], v[76:77]\n"
    "v_pk_add_f32 v[92:93], v[92:93], v[120:121]\n"
    "v_pk_add_f32 v[62:63], v[62:63], v[78:79]\n"
    "v_pk_add_f32 v[94:95], v[94:95], v[122:123]\n"
    "v_pk_add_f32 v[56:57], v[56:57], v[88:89]\n"
    "v_pk_add_f32 v[58:59], v[58:59], v[90:91]\n"
    "v_pk_add_f32 v[60:61], v[60:61], v[92:93]\n"
    "v_pk_add_f32 v[62:63], v[62:63], v[94:95]\n"
    "v_pk_mul_f32 v[72:73], v[56:57], v[124:125] op_sel_hi:[1,0]\n"
    "v_pk_mul_f32 v[74:75], v[58:59], v[124:125] op_sel_hi:[1,0]\n"
    "v_pk_mul_f32 v[76:77], v[60:61], v[124:125] op_sel_hi:[1,0]\n"
    "v_pk_mul_f32 v[78:79], v[62:63], v[124:125] op_sel_hi:[1,0]\n"
    "v_exp_f32 v72, v72\n"
    "v_exp_f32 v73, v73\n"
    "v_exp_f32 v74, v74\n"
    "v_exp_f32 v75, v75\n"
    "v_exp_f32 v76, v76\n"
    "v_exp_f32 v77, v77\n"
    "v_exp_f32 v78, v78\n"
    "v_exp_f32 v79, v79\n"
    "v_pk_mul_f32 v[56:57], v[56:57], v[40:41]\n"
    "v_pk_mul_f32 v[58:59], v[58:59], v[42:43]\n"
    "v_pk_mul_f32 v[60:61], v[60:61], v[44:45]\n"
    "v_pk_mul_f32 v[62:63], v[62:63], v[46:47]\n"
    "v_pk_add_f32 v[72:73], v[72:73], v[124:125] op_sel:[0,1]\n"
    "v_pk_add_f32 v[74:75], v[74:75], v[124:125] op_sel:[0,1]\n"
    "v_pk_add_f32 v[76:77], v[76:77], v[124:125] op_sel:[0,1]\n"
    "v_pk_add_f32 v[78:79], v[78:79], v[124:125] op_sel:[0,1]\n"
    "v_rcp_f32 v72, v72\n"
    "v_rcp_f32 v73, v73\n"
    "v_rcp_f32 v74, v74\n"
    "v_rcp_f32 v75, v75\n"
    "v_rcp_f32 v76, v76\n"
    "v_rcp_f32 v77, v77\n"
    "v_rcp_f32 v78, v78\n"
    "v_rcp_f32 v79, v79\n"
    "v_pk_fma_f32 v[56:57], v[56:57], v[72:73], v[240:241]\n"
    "v_pk_fma_f32 v[58:59], v[58:59], v[74:75], v[242:243]\n"
    "v_pk_fma_f32 v[60:61], v[60:61], v[76:77], v[244:245]\n"
    "v_pk_fma_f32 v[62:63], v[62:63], v[78:79], v[246:247]\n"
    "v_cvt_pk_bf16_f32 v88, v56, v57\n"
    "v_cvt_pk_bf16_f32 v89, v58, v59\n"
    "v_cvt_pk_bf16_f32 v90, v60, v61\n"
    "v_cvt_pk_bf16_f32 v91, v62, v63\n"
    "buffer_store_dwordx4 v[88:91], v23, s[24:27], 0 offen\n"
    "s_waitcnt lgkmcnt(0)\n"
    "v_pk_add_f32 v[64:65], v[64:65], v[80:81]\n"
    "v_pk_add_f32 v[96:97], v[96:97], v[116:117]\n"
    "v_pk_add_f32 v[66:67], v[66:67], v[82:83]\n"
    "v_pk_add_f32 v[98:99], v[98:99], v[118:119]\n"
    "v_pk_add_f32 v[68:69], v[68:69], v[84:85]\n"
    "v_pk_add_f32 v[100:101], v[100:101], v[120:121]\n"
    "v_pk_add_f32 v[70:71], v[70:71], v[86:87]\n"
    "v_pk_add_f32 v[102:103], v[102:103], v[122:123]\n"
    "v_pk_add_f32 v[64:65], v[64:65], v[96:97]\n"
    "v_pk_add_f32 v[66:67], v[66:67], v[98:99]\n"
    "v_pk_add_f32 v[68:69], v[68:69], v[100:101]\n"
    "v_pk_add_f32 v[70:71], v[70:71], v[102:103]\n"
    "v_pk_mul_f32 v[80:81], v[64:65], v[124:125] op_sel_hi:[1,0]\n"
    "v_pk_mul_f32 v[82:83], v[66:67], v[124:125] op_sel_hi:[1,0]\n"
    "v_pk_mul_f32 v[84:85], v[68:69], v[124:125] op_sel_hi:[1,0]\n"
    "v_pk_mul_f32 v[86:87], v[70:71], v[124:125] op_sel_hi:[1,0]\n"
    "v_exp_f32 v80, v80\n"
    "v_exp_f32 v81, v81\n"
    "v_exp_f32 v82, v82\n"
    "v_exp_f32 v83, v83\n"
    "v_exp_f32 v84, v84\n"
    "v_exp_f32 v85, v85\n"
    "v_exp_f32 v86, v86\n"
    "v_exp_f32 v87, v87\n"
    "v_pk_mul_f32 v[64:65], v[64:65], v[48:49]\n"
    "v_pk_mul_f32 v[66:67], v[66:67], v[50:51]\n"
    "v_pk_mul_f32 v[68:69], v[68:69], v[52:53]\n"
    "v_pk_mul_f32 v[70:71], v[70:71], v[54:55]\n"
    "v_pk_add_f32 v[80:81], v[80:81], v[124:125] op_sel:[0,1]\n"
    "v_pk_add_f32 v[82:83], v[82:83], v[124:125] op_sel:[0,1]\n"
    "v_pk_add_f32 v[84:85], v[84:85], v[124:125] op_sel:[0,1]\n"
    "v_pk_add_f32 v[86:87], v[86:87], v[124:125] op_sel:[0,1]\n"
    "v_rcp_f32 v80, v80\n"
    "v_rcp_f32 v81, v81\n"
    "v_rcp_f32 v82, v82\n"
    "v_rcp_f32 v83, v83\n"
    "v_rcp_f32 v84, v84\n"
    "v_rcp_f32 v85, v85\n"
    "v_rcp_f32 v86, v86\n"
    "v_rcp_f32 v87, v87\n"
    "v_pk_fma_f32 v[64:65], v[64:65], v[80:81], v[248:249]\n"
    "v_pk_fma_f32 v[66:67], v[66:67], v[82:83], v[250:251]\n"
    "v_pk_fma_f32 v[68:69], v[68:69], v[84:85], v[252:253]\n"
    "v_pk_fma_f32 v[70:71], v[70:71], v[86:87], v[254:255]\n"
    "v_cvt_pk_bf16_f32 v96, v64, v65\n"
    "v_cvt_pk_bf16_f32 v97, v66, v67\n"
    "v_cvt_pk_bf16_f32 v98, v68, v69\n"
    "v_cvt_pk_bf16_f32 v99, v70, v71\n"
    "buffer_store_dwordx4 v[96:99], v24, s[24:27], 0 offen\n"
    "s_or_b32 s48, s2, s3\n"
    "s_cbranch_scc0 L_bump\n"
    "s_endpgm\n"
    "L_gre_attn:\n"
    "s_mov_b32 s48, 0\n"
    "L_grl_attn:\n"
    "s_sleep 4\n"
    "buffer_load_dwordx2 v[108:109], v34, s[16:19], 0 offen sc1\n"
    "s_add_u32 s48, s48, 1\n"
    "s_waitcnt vmcnt(0)\n"
    "v_cmp_ne_u32 vcc, s66, v109\n"
    "s_cmp_gt_u32 s48, 20000\n"
    "s_cbranch_scc1 L_gok_attn\n"
    "s_cbranch_vccnz L_grl_attn\n"
    "s_branch L_gok_attn\n"
    "L_gre_ffn:\n"
    "s_mov_b32 s48, 0\n"
    "L_grl_ffn:\n"
    "s_sleep 4\n"
    "buffer_load_dwordx2 v[108:109], v34, s[16:19], 0 offen sc1\n"
    "s_add_u32 s48, s48, 1\n"
    "s_waitcnt vmcnt(0)\n"
    "v_cmp_ne_u32 vcc, s66, v109\n"
    "s_cmp_gt_u32 s48, 20000\n"
    "s_cbranch_scc1 L_gok_ffn\n"
    "s_cbranch_vccnz L_grl_ffn\n"
    "s_branch L_gok_ffn\n"
    "L_pre:\n"
    "s_mov_b32 s49, 0\n"
    "L_prl:\n"
    "s_sleep 2\n"
    "buffer_load_dwordx2 v[104:105], v16, s[16:19], 0 offen sc1\n"
    "s_add_u32 s49, s49, 1\n"
    "s_waitcnt vmcnt(0)\n"
    "v_cmp_eq_u64 vcc, s[66:67], v[104:105]\n"
    "s_cmp_gt_u32 s49, 20000\n"
    "s_cbranch_scc1 L_pok\n"
    "s_andn2_b32 s48, s65, vcc_lo\n"
    "s_cbranch_scc1 L_prl\n"
    "s_branch L_pok\n"
    "L_bump:\n"
    "s_mov_b32 s49, 0\n"
    "v_lshlrev_b32 v2, 7, v1\n"
    "v_add_u32 v2, 42008576, v2\n"
    "s_movk_i32 s28, 0x2000\n"
    "s_movk_i32 s29, 0x4000\n"
    "s_movk_i32 s30, 0x6000\n"
    "L_bul:\n"
    "buffer_load_dwordx2 v[4:5], v2, s[16:19], 0 offen sc1\n"
    "buffer_load_dwordx2 v[6:7], v2, s[16:19], s28 offen sc1\n"
    "buffer_load_dwordx2 v[8:9], v2, s[16:19], s29 offen sc1\n"
    "buffer_load_dwordx2 v[10:11], v2, s[16:19], s30 offen sc1\n"
    "s_add_u32 s49, s49, 1\n"
    "s_waitcnt vmcnt(0)\n"
    "v_cmp_ne_u64 s[4:5], s[66:67], v[4:5]\n"
    "v_cmp_ne_u64 s[6:7], s[66:67], v[6:7]\n"
    "v_cmp_ne_u64 s[8:9], s[66:67], v[8:9]\n"
    "v_cmp_ne_u64 vcc, s[66:67], v[10:11]\n"
    "s_cmp_gt_u32 s49, 20000\n"
    "s_cbranch_scc1 L_bud\n"
    "s_or_b64 s[4:5], s[4:5], s[6:7]\n"
    "s_or_b64 s[8:9], s[8:9], vcc\n"
    "s_or_b64 s[4:5], s[4:5], s[8:9]\n"
    "s_cbranch_scc1 L_bul\n"
    "L_bud:\n"
    "v_mov_b32 v4, s66\n"
    "v_mov_b32 v5, s67\n"
    "v_mov_b32 v2, 0x7fffff00\n"
    "v_mov_b32 v3, 41943040\n"
    "v_cmp_eq_u32 vcc, 0, v1\n"
    "s_nop 1\n"
    "v_cndmask_b32 v2, v2, v3, vcc\n"
    "buffer_store_dwordx2 v[4:5], v2, s[16:19], 0 offen sc1\n"
    "s_endpgm\n"
    "L_end:\n"
    :
    : "{s[0:1]}"(__builtin_amdgcn_kernarg_segment_ptr()), "{s2}"(__builtin_amdgcn_workgroup_id_x()),
      "{v0}"(__builtin_amdgcn_workitem_id_x()),
      "{s3}"((unsigned)(size_t)(__attribute__((address_space(3))) char*)smem)
    : "memory", "v255", "a255", "s99", "vcc");
}

extern "C" void kernel_launch(void* const* d_in, const int* in_sizes, int n_in, void* d_out, int out_size,
                              void* d_ws, size_t ws_size, hipStream_t stream) {
  FusedArgs a{d_in[0], d_ws, d_in[1], d_in[4], d_in[2], d_in[6], d_in[5], d_in[7], d_out};
  fused_block_kernel<<<256, 256, 0, stream>>>(a);
}
```
